```python
import torch

def _softmax(x, axis=-1):
    return torch.softmax(x, dim=axis)
def _gelu(x):
    return torch.nn.functional.gelu(x, approximate="tanh")
def _silu(x):
    return torch.nn.functional.silu(x)
def _sigmoid(x):
    return torch.sigmoid(x)
def _layernorm(x, axis=-1, eps=1e-5):
    m = x.mean(dim=axis, keepdim=True)
    v = x.var(dim=axis, keepdim=True, unbiased=False)
    return (x - m) / torch.sqrt(v + eps)
def _scatter_add(tgt, idx, src, axis=0):
    return tgt.clone().index_add_(axis, idx, src)
def _segment_sum(src, seg, n):
    out = torch.zeros((n,) + src.shape[1:], dtype=src.dtype)
    return out.index_add_(0, seg, src)
def _pad(x, pw):
    flat = tuple(v for pair in reversed(pw) for v in pair)
    return torch.nn.functional.pad(x, flat)

kernel_name = 'recurrent_scan__ssm_conv_s4__dense_activation_mish__prompt'

def setup_inputs():

    _w = (lambda a: (lambda t: t.to(torch.bfloat16) if t.is_floating_point() else t)(torch.as_tensor(a)))
    def _A_setup():
        B, L, D, N = 1, 2048, 640, 192
        A_re = -(lambda _x: torch.exp(torch.as_tensor(_x)))((lambda *s: torch.randn(*s, dtype=torch.bfloat16) * 3.2)(D, N))
        A_im = torch.pi * (lambda *s: torch.randn(*s, dtype=torch.bfloat16) * 0.2)(D, N)
        B_re = (lambda *s: torch.randn(*s, dtype=torch.bfloat16) * 0.2)(D, N)
        B_im = (lambda *s: torch.randn(*s, dtype=torch.bfloat16) * 0.2)(D, N)
        C_re = (lambda *s: torch.randn(*s, dtype=torch.bfloat16) * 0.2)(D, N)
        C_im = (lambda *s: torch.randn(*s, dtype=torch.bfloat16) * 0.2)(D, N)
        log_dt = (lambda *s: torch.randn(*s, dtype=torch.bfloat16) * 0.2)(D) * 0.5 - 4.0
        Dskip = (lambda *s: torch.randn(*s, dtype=torch.bfloat16) * 0.2)(D)
        return {
            "u": _w((lambda *s: torch.randn(*s, dtype=torch.bfloat16) * 0.05)(B, L, D)),
            "A_re": _w(A_re),
            "A_im": _w(A_im),
            "B_re": _w(B_re),
            "B_im": _w(B_im),
            "C_re": _w(C_re),
            "C_im": _w(C_im),
            "log_dt": _w(log_dt),
            "Dskip": _w(Dskip),
        }
    _A_in = _A_setup()

    def _B_setup():
        B, S, D, F = 1, 2048, 640, 1536
        return {
            "x": _w((lambda *s: torch.randn(*s, dtype=torch.bfloat16) * 0.05)(B, S, D)),
            "w1": _w((lambda *s: torch.randn(*s, dtype=torch.bfloat16) * 0.8)(D, F) * (D ** -0.5)),
            "b1": _w((lambda *s: torch.randn(*s, dtype=torch.bfloat16) * 0.1)(F)),
            "w2": _w((lambda *s: torch.randn(*s, dtype=torch.bfloat16) * 0.1)(F, D) * (F ** -0.5)),
        }
    _B_in = _B_setup()

    return {
        "A_u": _A_in["u"],
        "A_A_re": _A_in["A_re"],
        "A_A_im": _A_in["A_im"],
        "A_B_re": _A_in["B_re"],
        "A_B_im": _A_in["B_im"],
        "A_C_re": _A_in["C_re"],
        "A_C_im": _A_in["C_im"],
        "A_log_dt": _A_in["log_dt"],
        "A_Dskip": _A_in["Dskip"],
        "B_x": _B_in["x"],
        "B_w1": _B_in["w1"],
        "B_b1": _B_in["b1"],
        "B_w2": _B_in["w2"],
    }

def reference(A_u, A_A_re, A_A_im, A_B_re, A_B_im, A_C_re, A_C_im, A_log_dt, A_Dskip, B_x, B_w1, B_b1, B_w2):
    def _A_ref(u, A_re, A_im, B_re, B_im, C_re, C_im, log_dt, Dskip):
        Bsz, L, D = u.shape
        dt = torch.exp(log_dt)[:, None]
        A = A_re + 1j * A_im
        Bc = B_re + 1j * B_im
        Cc = C_re + 1j * C_im
        logdA = dt * A
        dB = dt * Bc
        l = (lambda x, dt: x.to(dt))(torch.arange(L), A_re.dtype)
        P = torch.exp(logdA[:, :, None] * l[None, None, :])
        K = torch.einsum("dn,dnl,dn->dl", Cc, P, dB).real
        L2 = 2 * L
        Kp = _pad(K, ((0, 0), (0, L)))
        up = (lambda x, *d: x.permute(*d))(u, 0, 2, 1)
        up = _pad(up, ((0, 0), (0, 0), (0, L)))
        Kf = (lambda x, axes=(-2,-1): torch.fft.rfft2(x.float(), dim=axes))(Kp[:, None, :], axes=(-2, -1))
        Uf = (lambda x, axes=(-2,-1): torch.fft.rfft2(x.float(), dim=axes))(up[:, :, None, :], axes=(-2, -1))
        Yf = Uf * Kf[None, :, :, :]
        y = (lambda x, s, axes=(-2,-1): torch.fft.irfft2(x, s=s, dim=axes).to(torch.bfloat16))(Yf, (1, L2), axes=(-2, -1))[:, :, 0, :L]
        y = (lambda x, *d: x.permute(*d))(y, 0, 2, 1) + u * Dskip[None, None, :]
        return y

    def _B_ref(x, w1, b1, w2):
        h = torch.einsum("bsd,df->bsf", x, w1) + b1
        sp = torch.log1p(torch.exp(-torch.abs(h))) + torch.where(h > 0, h, torch.zeros_like(h))
        act = h * torch.tanh(sp)
        return torch.einsum("bsf,fd->bsd", act, w2)

    x = A_u
    xn = _layernorm(x, axis=-1)
    s = _A_ref(xn, A_A_re, A_A_im, A_B_re, A_B_im, A_C_re, A_C_im, A_log_dt, A_Dskip)
    g = _silu(_B_ref(xn, B_w1, B_b1, B_w2))
    return x + s * g
```

```cpp
#include <hip/hip_runtime.h>
#include <hip/hip_bf16.h>
typedef unsigned short u16;
constexpr int L = 2048, D = 640, N = 192, F = 1536;
__device__ __forceinline__ float bf2f(u16 b) { return __uint_as_float(((unsigned)b) << 16); }
__device__ __forceinline__ u16 f2bf(float f) { __hip_bfloat16 h = __float2bfloat16(f); return *(u16*)&h; }
__device__ __forceinline__ float rbf(float f) { return bf2f(f2bf(f)); }

__global__ void k_ln(const u16* x, u16* xn) {
  int t = blockIdx.x, lane = threadIdx.x;
  float v[10]; float s = 0;
  for (int i = 0; i < 10; ++i) { v[i] = bf2f(x[t * D + lane + 64 * i]); s += v[i]; }
  for (int o = 32; o; o >>= 1) s += __shfl_xor(s, o);
  float m = s / D; float q = 0;
  for (int i = 0; i < 10; ++i) { float d = v[i] - m; q += d * d; }
  for (int o = 32; o; o >>= 1) q += __shfl_xor(q, o);
  float mb = rbf(m), vb = rbf(q / D);
  float den = rbf(sqrtf(rbf(vb + 1e-5f)));
  for (int i = 0; i < 10; ++i) xn[t * D + lane + 64 * i] = f2bf(rbf(v[i] - mb) / den);
}
__global__ void k_kgen(const u16* Are, const u16* Aim, const u16* Bre, const u16* Bim, const u16* Cre, const u16* Cim,
                       const u16* logdt, float* K) {
  int d = blockIdx.x;
  __shared__ float a[N], b[N], wr[N], wi[N];
  float dt = rbf(expf(bf2f(logdt[d])));
  for (int n = threadIdx.x; n < N; n += blockDim.x) {
    a[n] = dt * bf2f(Are[d * N + n]); b[n] = dt * bf2f(Aim[d * N + n]);
    float br = dt * bf2f(Bre[d * N + n]), bi = dt * bf2f(Bim[d * N + n]);
    float cr = bf2f(Cre[d * N + n]), ci = bf2f(Cim[d * N + n]);
    wr[n] = cr * br - ci * bi; wi[n] = cr * bi + ci * br;
  }
  __syncthreads();
  for (int l = threadIdx.x; l < L; l += blockDim.x) {
    float lf = rbf((float)l); float acc = 0;
    for (int n = 0; n < N; ++n) {
      float e = expf(a[n] * lf); float sn, cs; sincosf(b[n] * lf, &sn, &cs);
      acc += e * (wr[n] * cs - wi[n] * sn);
    }
    K[d * L + l] = acc;
  }
}
__global__ void k_conv(const u16* xn, const float* K, const u16* Dskip, u16* s) {
  int d = blockIdx.x;
  __shared__ float u[L], k[L];
  for (int t = threadIdx.x; t < L; t += blockDim.x) { u[t] = bf2f(xn[t * D + d]); k[t] = K[d * L + t]; }
  __syncthreads();
  float ds = bf2f(Dskip[d]);
  for (int t = threadIdx.x; t < L; t += blockDim.x) {
    float acc = 0;
    for (int l = 0; l <= t; ++l) acc += k[l] * u[t - l];
    s[t * D + d] = f2bf(rbf(acc) + rbf(u[t] * ds));
  }
}
__global__ void k_g1(const u16* xn, const u16* w1, const u16* b1, u16* act) {
  int t = blockIdx.x; __shared__ float r[D];
  for (int i = threadIdx.x; i < D; i += blockDim.x) r[i] = bf2f(xn[t * D + i]);
  __syncthreads();
  for (int f = threadIdx.x; f < F; f += blockDim.x) {
    float acc = 0;
    for (int i = 0; i < D; ++i) acc += r[i] * bf2f(w1[i * F + f]);
    float h = rbf(rbf(acc) + bf2f(b1[f]));
    float e = __expf(-fabsf(h));
    float num = h > 0 ? 1.f + 2.f * e : e * e + 2.f * e;
    float den = h > 0 ? num + 2.f * e * e : num + 2.f;
    act[t * F + f] = f2bf(h * num / den);
  }
}
__global__ void k_g2(const u16* act, const u16* w2, const u16* x, const u16* s, u16* out) {
  int t = blockIdx.x; __shared__ float r[F];
  for (int i = threadIdx.x; i < F; i += blockDim.x) r[i] = bf2f(act[t * F + i]);
  __syncthreads();
  for (int d = threadIdx.x; d < D; d += blockDim.x) {
    float acc = 0;
    for (int i = 0; i < F; ++i) acc += r[i] * bf2f(w2[i * D + d]);
    float o = rbf(acc); float g = rbf(o / (1.f + __expf(-o)));
    out[t * D + d] = f2bf(bf2f(x[t * D + d]) + rbf(bf2f(s[t * D + d]) * g));
  }
}
extern "C" void kernel_launch(void* const* d_in, const int* in_sizes, int n_in, void* d_out, int out_size, void* d_ws,
                              size_t ws_size, hipStream_t stream) {
  const u16* x = (const u16*)d_in[0];
  char* ws = (char*)d_ws;
  u16* xn = (u16*)ws; float* K = (float*)(ws + (4 << 20)); u16* s = (u16*)(ws + (12 << 20)); u16* act = (u16*)(ws + (16 << 20));
  k_ln<<<L, 64, 0, stream>>>(x, xn);
  k_kgen<<<D, 256, 0, stream>>>((const u16*)d_in[1], (const u16*)d_in[2], (const u16*)d_in[3], (const u16*)d_in[4],
                                (const u16*)d_in[5], (const u16*)d_in[6], (const u16*)d_in[7], K);
  k_conv<<<D, 256, 0, stream>>>(xn, K, (const u16*)d_in[8], s);
  k_g1<<<L, 256, 0, stream>>>(xn, (const u16*)d_in[10], (const u16*)d_in[11], act);
  k_g2<<<L, 256, 0, stream>>>(act, (const u16*)d_in[12], x, s, (u16*)d_out);
}
```

```cpp
#include <hip/hip_runtime.h>
#define FUSED 0
typedef unsigned short u16;
typedef unsigned u32;
typedef unsigned long long u64;
typedef u32 v4u __attribute__((ext_vector_type(4)));
typedef u32 v2u __attribute__((ext_vector_type(2)));
typedef float f2 __attribute__((ext_vector_type(2)));
typedef float f4 __attribute__((ext_vector_type(4)));
typedef float f16v __attribute__((ext_vector_type(16)));
typedef short s4 __attribute__((ext_vector_type(4)));
typedef __bf16 bf2 __attribute__((ext_vector_type(2)));
typedef __bf16 bf8 __attribute__((ext_vector_type(8)));
#define LAS __attribute__((address_space(3)))
#define GAS __attribute__((address_space(1)))
#define DI __device__ __forceinline__

constexpr int L = 2048, D = 640, NS = 192, F = 1536;
constexpr u32 WS_CTRL = 0, WS_XNT = 65536, WS_W2T = WS_XNT + L * D * 2, WS_ACT = WS_W2T + F * D * 2,
              WS_ST = WS_ACT + L * F * 2, WS_END = WS_ST + L * D * 2;
constexpr int LDS_BYTES = 80 * 1024;
constexpr int AUX_SC1 = 16;

struct Params {
  const u16 *x, *Are, *Aim, *Bre, *Bim, *Cre, *Cim, *logdt, *dskip, *w1, *b1, *w2;
  u16* out;
  unsigned char* ws;
};

#define STAMP(i)
#define STP
#define STA
DI float bflo(u32 v) { return __builtin_bit_cast(float, v << 16); }
DI float bfhi(u32 v) { return __builtin_bit_cast(float, v & 0xffff0000u); }
DI float bf2f(u16 v) { return __builtin_bit_cast(float, (u32)v << 16); }
DI u32 pk(float a, float b) { f2 t = {a, b}; return __builtin_bit_cast(u32, __builtin_convertvector(t, bf2)); }
DI float rbf(float a) { return bflo(pk(a, a)); }
DI f4 mfma16(v4u a, v4u b, f4 c) { return __builtin_amdgcn_mfma_f32_16x16x32_bf16(__builtin_bit_cast(bf8, a), __builtin_bit_cast(bf8, b), c, 0, 0, 0); }
DI f16v mfma32(v4u a, v4u b, f16v c) { return __builtin_amdgcn_mfma_f32_32x32x16_bf16(__builtin_bit_cast(bf8, a), __builtin_bit_cast(bf8, b), c, 0, 0, 0); }
DI float dot2(u32 a, u32 b, float c) { return __builtin_amdgcn_fdot2_f32_bf16(__builtin_bit_cast(bf2, a), __builtin_bit_cast(bf2, b), c, false); }
DI v2u trd(LAS unsigned char* p) { return __builtin_bit_cast(v2u, __builtin_amdgcn_ds_read_tr16_b64_v4i16((LAS s4*)p)); }
DI v4u mkrsrc(const void* p, u32 bytes) {
  u64 a = (u64)p;
  return v4u{(u32)a, (u32)(a >> 32) & 0xffffu, bytes, 0x00020000u};
}
DI void glds(u32 ldsaddr, u32 voff, v4u rs, u32 soff) {
  asm volatile("s_mov_b32 m0, %0\n\ts_nop 0\n\tbuffer_load_dwordx4 %1, %2, %3 offen lds" ::"s"(ldsaddr), "v"(voff), "s"(rs), "s"(soff) : "memory");
}
#define WAITV(n) asm volatile("s_waitcnt vmcnt(" #n ")" ::: "memory")
#define WAITL0() __builtin_amdgcn_s_waitcnt(0xC07F)
#define BAR() __builtin_amdgcn_s_barrier()
#define SB() __builtin_amdgcn_sched_barrier(0)
template <int CTRL> DI float dppadd(float v) {
  return v + __builtin_bit_cast(float, __builtin_amdgcn_update_dpp(0, __builtin_bit_cast(int, v), CTRL, 0xf, 0xf, true));
}
DI float sum32x(float v) {
  u32 u = __builtin_bit_cast(u32, v), u2 = u;
  asm volatile("" : "+v"(u2));
  const v2u r = __builtin_amdgcn_permlane32_swap(u, u2, false, false);
  const u32 a = r.x, b = r.y;
  return __builtin_bit_cast(float, a) + __builtin_bit_cast(float, b);
}
DI float sum16x(float v) {
  u32 u = __builtin_bit_cast(u32, v), u2 = u;
  asm volatile("" : "+v"(u2));
  const v2u r = __builtin_amdgcn_permlane16_swap(u, u2, false, false);
  const u32 a = r.x, b = r.y;
  return __builtin_bit_cast(float, a) + __builtin_bit_cast(float, b);
}
DI float wavesum(float v) {
  v = dppadd<0xB1>(v);
  v = dppadd<0x4E>(v);
  v = dppadd<0x141>(v);
  v = dppadd<0x140>(v);
  return sum32x(sum16x(v));
}
DI f2 cmul(f2 x, f2 m, f2 m2) { return __builtin_elementwise_fma(x.yy, m2, x.xx * m); }

constexpr u32 G_W = 0, G_X = 49152, G_CS = 65536, G_B1 = G_CS + 768, G_SX = G_B1 + 768;
struct Frags { v4u x[2]; v4u w[2][3]; };

template <int S4, bool LAST>
DI void g1_stage(LAS unsigned char* lds, u32 ldsb, Frags& cur, Frags& prev, f16v (&acc)[3], float& sx, float& sxx, float (&cs)[3],
                 const u32 (&xo)[4], const u32 (&wo)[6], u32 voffX, const u32 (&voffW)[3], v4u rsX, v4u rsW, u32& xk, u32& wk,
                 u32 xrow, u32 wcol, int w, bool first) {
  if (!LAST) { if (S4 & 1) WAITV(8); else WAITV(6); }
  else { if (S4 == 0) WAITV(6); else if (S4 == 1) WAITV(8); else if (S4 == 2) WAITV(3); else WAITV(0); }
  WAITL0();
  BAR();
  SB();
  if (!LAST || S4 == 0) {
    if (!(S4 & 1)) {
      const u32 dst = ldsb + G_X + (((S4 >> 1) + 1) & 1) * 8192 + w * 1024;
      glds(dst, voffX, rsX, xrow + xk);
      glds(dst + 4096, voffX, rsX, xrow + 32 * 1280 + xk);
      xk += 128; if (xk == 1280) xk = 0;
    }
    const u32 dst = ldsb + G_W + ((S4 + 3) & 3) * 12288 + w * 3072;
#pragma unroll
    for (int i = 0; i < 3; ++i) glds(dst + i * 1024, voffW[i], rsW, wcol + wk);
    wk += 32 * 3072; if (wk == 640 * 3072) wk = 0;
  }
#pragma unroll
  for (int k = 0; k < 2; ++k) {
    cur.x[k] = *(LAS v4u*)(lds + xo[2 * (S4 & 1) + k] + (S4 >> 1) * 8192);
#pragma unroll
    for (int ft = 0; ft < 3; ++ft) {
      v2u a = trd(lds + wo[k * 3 + ft] + S4 * 12288), b = trd(lds + wo[k * 3 + ft] + S4 * 12288 + 1536);
      cur.w[k][ft] = v4u{a.x, a.y, b.x, b.y};
    }
  }
  if (!first) {
#pragma unroll
    for (int k = 0; k < 2; ++k)
#pragma unroll
      for (int ft = 0; ft < 3; ++ft) acc[ft] = mfma32(prev.w[k][ft], prev.x[k], acc[ft]);
#pragma unroll
    for (int e = 0; e < 4; ++e) {
      sx = dot2(prev.x[0][e], 0x3f803f80u, sx);
      sxx = dot2(prev.x[0][e], prev.x[0][e], sxx);
#pragma unroll
      for (int ft = 0; ft < 3; ++ft) cs[ft] = dot2(prev.w[0][ft][e], 0x3f803f80u, cs[ft]);
    }
  }
  SB();
}

DI void gemm1(const Params& p, LAS unsigned char* lds, int m, int j, int tid STP) {
  const int lane = tid & 63, w = __builtin_amdgcn_readfirstlane(tid >> 6), wm = w >> 1, wn = w & 1, fst = wm ^ wn;
  const u32 ldsb = (u32)(size_t)lds;
  const v4u rsX = mkrsrc(p.x, L * D * 2), rsW = mkrsrc(p.w1, D * F * 2);
  const u32 voffX = (8 * w + (lane >> 3)) * 1280 + 16 * ((lane & 7) ^ (4 * (w & 1) + (lane >> 4)));
  u32 voffW[3];
#pragma unroll
  for (int i = 0; i < 3; ++i) {
    const u32 z = (3 * w + i) * 64 + lane, row = z / 24, pos = z % 24;
    voffW[i] = row * 3072 + ((((pos >> 2) ^ ((row >> 1) & 1)) << 2) + (pos & 3)) * 16;
  }
  const int rot = ((m & 3) * 5 + j * 2 + (j >> 2)) % 10;
  u32 xk = rot * 128, wk = rot * 64 * 3072;
  const u32 xrow = m * 64 * 1280, wcol = j * 384;
  glds(ldsb + G_X + w * 1024, voffX, rsX, xrow + xk);
  glds(ldsb + G_X + 4096 + w * 1024, voffX, rsX, xrow + 32 * 1280 + xk);
  xk += 128; if (xk == 1280) xk = 0;
#pragma unroll
  for (int s = 0; s < 3; ++s) {
#pragma unroll
    for (int i = 0; i < 3; ++i) glds(ldsb + G_W + s * 12288 + w * 3072 + i * 1024, voffW[i], rsW, wcol + wk);
    wk += 32 * 3072; if (wk == 640 * 3072) wk = 0;
  }
  if (tid < 192) { *(LAS float*)(lds + G_CS + tid * 4) = 0.f; }
  else { *(LAS f2*)(lds + G_SX + (tid - 192) * 8) = f2{0.f, 0.f}; }
  const int tok = lane & 31, hh = lane >> 5;
  u32 xo[4], wo[6];
  {
    const u32 row = 32 * wm + tok, b = G_X + row * 128 + 16 * (hh ^ ((row >> 1) & 7));
    xo[0] = b ^ (32 * fst); xo[1] = b ^ (32 * (1 - fst)); xo[2] = xo[0] ^ 64; xo[3] = xo[1] ^ 64;
    const u32 q4 = (lane & 15) >> 2, blk = (lane >> 4) & 1;
#pragma unroll
    for (int ft = 0; ft < 3; ++ft) {
      const u32 a = G_W + (8 * hh + q4) * 384 + 64 * ((3 * wn + ft) ^ (q4 >> 1)) + 32 * blk + 8 * (lane & 3);
      wo[ft] = a + 6144 * fst; wo[3 + ft] = a + 6144 * (1 - fst);
    }
  }
  f16v acc[3] = {};
  float sx = 0.f, sxx = 0.f, cs[3] = {0.f, 0.f, 0.f};
  Frags A, B;
#define ARGS acc, sx, sxx, cs, xo, wo, voffX, voffW, rsX, rsW, xk, wk, xrow, wcol, w
  B.x[0] = B.x[1] = v4u{0, 0, 0, 0};
#pragma unroll
  for (int k = 0; k < 2; ++k)
#pragma unroll
    for (int ft = 0; ft < 3; ++ft) B.w[k][ft] = v4u{0, 0, 0, 0};
  STAMP(1);
#pragma unroll 1
  for (int it = 0; it < 4; ++it) {
    g1_stage<0, false>(lds, ldsb, A, B, ARGS, false);
    if (it == 0) STAMP(2);
    g1_stage<1, false>(lds, ldsb, B, A, ARGS, false);
    g1_stage<2, false>(lds, ldsb, A, B, ARGS, false);
    g1_stage<3, false>(lds, ldsb, B, A, ARGS, false);
  }
  g1_stage<0, true>(lds, ldsb, A, B, ARGS, false);
  g1_stage<1, true>(lds, ldsb, B, A, ARGS, false);
  g1_stage<2, true>(lds, ldsb, A, B, ARGS, false);
  g1_stage<3, true>(lds, ldsb, B, A, ARGS, false);
#undef ARGS
  STAMP(3);
  if (tid < 192) *(LAS float*)(lds + G_B1 + tid * 4) = bf2f(p.b1[j * 192 + tid]);
#pragma unroll
  for (int k = 0; k < 2; ++k)
#pragma unroll
    for (int ft = 0; ft < 3; ++ft) acc[ft] = mfma32(B.w[k][ft], B.x[k], acc[ft]);
#pragma unroll
  for (int e = 0; e < 4; ++e) {
    sx = dot2(B.x[0][e], 0x3f803f80u, sx);
    sxx = dot2(B.x[0][e], B.x[0][e], sxx);
#pragma unroll
    for (int ft = 0; ft < 3; ++ft) cs[ft] = dot2(B.w[0][ft][e], 0x3f803f80u, cs[ft]);
  }
  sx = sum32x(sx); sxx = sum32x(sxx);
#pragma unroll
  for (int ft = 0; ft < 3; ++ft) cs[ft] = sum32x(cs[ft]);
  if (hh == 0) {
    LAS float* sp = (LAS float*)(lds + G_SX) + (32 * wm + tok) * 2;
    __hip_atomic_fetch_add(sp, sx, __ATOMIC_RELAXED, __HIP_MEMORY_SCOPE_WORKGROUP);
    __hip_atomic_fetch_add(sp + 1, sxx, __ATOMIC_RELAXED, __HIP_MEMORY_SCOPE_WORKGROUP);
#pragma unroll
    for (int ft = 0; ft < 3; ++ft)
      __hip_atomic_fetch_add((LAS float*)(lds + G_CS) + 96 * wn + 32 * ft + tok, cs[ft], __ATOMIC_RELAXED, __HIP_MEMORY_SCOPE_WORKGROUP);
  }
  __syncthreads();
  STAMP(4);
  const f2 st = *(LAS f2*)(lds + G_SX + (32 * wm + tok) * 8);
  const float mean = st.x * (1.f / D), var = fmaxf(st.y * (1.f / D) - mean * mean, 0.f);
  const float den = rbf(__builtin_amdgcn_sqrtf(rbf(rbf(var) + 1e-5f)));
  const float rinv = __builtin_amdgcn_rcpf(den), nmu = -rbf(mean) * rinv;
  const __amdgpu_buffer_rsrc_t rsA = __builtin_amdgcn_make_buffer_rsrc(p.ws + WS_ACT, 0, L * F * 2, 0x00020000);
  const u32 aoff = ((2 * wm + (tok >> 4)) * 64 + 32 * hh + (tok & 15)) * 16;
#pragma unroll
  for (int ft = 0; ft < 3; ++ft) {
    u32 o[8];
#pragma unroll
    for (int q = 0; q < 4; ++q) {
      const u32 fo = (96 * wn + 32 * ft + 8 * q + 4 * hh) * 4;
      const f4 c4 = *(LAS f4*)(lds + G_CS + fo), b4 = *(LAS f4*)(lds + G_B1 + fo);
#pragma unroll
      for (int e = 0; e < 2; ++e) {
        const f2 a2 = {acc[ft][4 * q + 2 * e], acc[ft][4 * q + 2 * e + 1]};
        const f2 c2 = {c4[2 * e], c4[2 * e + 1]}, b2 = {b4[2 * e], b4[2 * e + 1]};
        const f2 h = __builtin_elementwise_fma(a2, f2{rinv, rinv}, __builtin_elementwise_fma(c2, f2{nmu, nmu}, b2));
        const f2 z = h * 1.44269504f;
        const f2 ex = {__builtin_amdgcn_exp2f(fminf(z.x, 60.f)), __builtin_amdgcn_exp2f(fminf(z.y, 60.f))};
        const f2 t = ex + 2.f, n = ex * t, d = __builtin_elementwise_fma(ex, t, f2{2.f, 2.f});
        const f2 r = {__builtin_amdgcn_rcpf(d.x), __builtin_amdgcn_rcpf(d.y)};
        const f2 a = h * (n * r);
        o[2 * q + e] = pk(a.x, a.y);
      }
    }
    const u32 so = (m * 48 + j * 6 + 3 * wn + ft) * 4096;
    __builtin_amdgcn_raw_buffer_store_b128(v4u{o[0], o[1], o[2], o[3]}, rsA, aoff, so, FUSED ? AUX_SC1 : 0);
    __builtin_amdgcn_raw_buffer_store_b128(v4u{o[4], o[5], o[6], o[7]}, rsA, aoff + 256, so, FUSED ? AUX_SC1 : 0);
  }
}

constexpr u32 G_RED = 0, G_S = 61440;
DI void gemm2(const Params& p, LAS unsigned char* lds, int m, int j, int tid STP) {
  const int lane = tid & 63, w = __builtin_amdgcn_readfirstlane(tid >> 6);
  const __amdgpu_buffer_rsrc_t rs = __builtin_amdgcn_make_buffer_rsrc(p.ws, 0, WS_END, 0x00020000);
  constexpr int AX = FUSED ? AUX_SC1 : 0;
  const u32 wb = WS_W2T + (j * 48 + 12 * w) * 5120, ab = WS_ACT + (m * 48 + 12 * w) * 4096, vo = lane * 16;
  v4u wf[6][5], af[2][4];
#pragma unroll
  for (int k = 0; k < 6; ++k)
#pragma unroll
    for (int n = 0; n < 5; ++n) wf[k][n] = __builtin_amdgcn_raw_buffer_load_b128(rs, vo + (n & 3) * 1024, wb + k * 5120 + (n >> 2) * 4096, AX);
  SB();
#pragma unroll
  for (int k = 0; k < 2; ++k)
#pragma unroll
    for (int t = 0; t < 4; ++t) af[k][t] = __builtin_amdgcn_raw_buffer_load_b128(rs, vo + t * 1024, ab + k * 4096, AX);
  SB();
  f4 acc[4][5] = {};
  STAMP(1);
#pragma unroll
  for (int k = 0; k < 12; ++k) {
    if (k == 1) STAMP(2);
    if (k == 6) STAMP(3);
#pragma unroll
    for (int t = 0; t < 4; ++t)
#pragma unroll
      for (int n = 0; n < 5; ++n) acc[t][n] = mfma16(wf[k % 6][n], af[k & 1][t], acc[t][n]);
    SB();
    if (k + 2 < 12) {
#pragma unroll
      for (int t = 0; t < 4; ++t) af[k & 1][t] = __builtin_amdgcn_raw_buffer_load_b128(rs, vo + t * 1024, ab + (k + 2) * 4096, AX);
    }
    if (k + 6 < 12) {
#pragma unroll
      for (int n = 0; n < 5; ++n) wf[k % 6][n] = __builtin_amdgcn_raw_buffer_load_b128(rs, vo + (n & 3) * 1024, wb + (k + 6) * 5120 + (n >> 2) * 4096, AX);
    }
    SB();
  }
  STAMP(4);
  v4u sv[3], xv[3];
#pragma unroll
  for (int i = 0; i < 3; ++i) {
    const u32 q = i * 256 + tid;
    if (q < 640) sv[i] = __builtin_amdgcn_raw_buffer_load_b128(rs, (80 * j + (q >> 3)) * 4096 + (q & 7) * 16, WS_ST + m * 128, AX);
    const u32 z = i * 64 + lane;
    if (z < 160) xv[i] = *(const GAS v4u*)((const GAS unsigned char*)p.x + (size_t)(64 * m + 16 * w + z / 10) * 1280 + j * 160 + (z % 10) * 16);
  }
  f4 own[5];
#pragma unroll
  for (int t = 0; t < 4; ++t) {
    if (t == w) {
#pragma unroll
      for (int n = 0; n < 5; ++n) own[n] = acc[t][n];
    } else {
      const int src = (w - t - 1) & 3;
#pragma unroll
      for (int n = 0; n < 5; ++n) *(LAS f4*)(lds + G_RED + ((t * 3 + src) * 5 + n) * 1024 + lane * 16) = acc[t][n];
    }
  }
#pragma unroll
  for (int i = 0; i < 3; ++i) {
    const u32 q = i * 256 + tid;
    if (q < 640) *(LAS v4u*)(lds + G_S + (q >> 3) * 160 + (q & 7) * 16) = sv[i];
  }
  __syncthreads();
  STAMP(5);
#pragma unroll
  for (int s = 0; s < 3; ++s)
#pragma unroll
    for (int n = 0; n < 5; ++n) own[n] += *(LAS f4*)(lds + G_RED + ((w * 3 + s) * 5 + n) * 1024 + lane * 16);
  const int g = lane >> 4;
  LAS unsigned char* P = lds + G_RED + w * 15360;
  v2u pr[5];
#pragma unroll
  for (int n = 0; n < 5; ++n) {
    const v2u s2 = trd(lds + G_S + (16 * n + 4 * g + ((lane & 15) >> 2)) * 160 + (16 * w + 4 * (lane & 3)) * 2);
    float gv[4];
#pragma unroll
    for (int e = 0; e < 4; ++e) {
      const float o = own[n][e];
      gv[e] = o * __builtin_amdgcn_rcpf(1.f + __builtin_amdgcn_exp2f(-1.44269504f * o));
    }
    pr[n] = v2u{pk(bflo(s2.x) * gv[0], bfhi(s2.x) * gv[1]), pk(bflo(s2.y) * gv[2], bfhi(s2.y) * gv[3])};
  }
  WAITL0();
#pragma unroll
  for (int n = 0; n < 5; ++n) *(LAS v2u*)(P + (lane & 15) * 176 + (16 * n + 4 * g) * 2) = pr[n];
#pragma unroll
  for (int i = 0; i < 3; ++i) {
    const u32 z = i * 64 + lane;
    if (z < 160) {
      const v4u q = *(LAS v4u*)(P + (z / 10) * 176 + (z % 10) * 16);
      v4u o;
#pragma unroll
      for (int e = 0; e < 4; ++e) o[e] = pk(bflo(xv[i][e]) + bflo(q[e]), bfhi(xv[i][e]) + bfhi(q[e]));
      *(GAS v4u*)((GAS unsigned char*)p.out + (size_t)(64 * m + 16 * w + z / 10) * 1280 + j * 160 + (z % 10) * 16) = o;
    }
  }
}

constexpr u32 C_TAB = 0, C_TABSZ = 14464, C_U = 3 * C_TABSZ, C_USZ = 4608, C_LN = C_U + 3 * C_USZ, C_W2 = C_LN + 10240, C_END = C_W2 + 8192;
static_assert(C_END <= LDS_BYTES, "lds");
constexpr u32 T_RA = 0, T_RB = 4160, T_CMP = 8320;

DI void ln_part(const Params& p, LAS unsigned char* lds, int c, int w, int lane) {
  const GAS u32* xr = (const GAS u32*)(p.x + (size_t)(8 * c + 2 * w) * D);
  u32 a[5], b[5];
#pragma unroll
  for (int i = 0; i < 5; ++i) { a[i] = xr[lane + 64 * i]; b[i] = xr[320 + lane + 64 * i]; }
  float sa = 0.f, sb = 0.f;
#pragma unroll
  for (int i = 0; i < 5; ++i) { sa += bflo(a[i]) + bfhi(a[i]); sb += bflo(b[i]) + bfhi(b[i]); }
  const float ma = wavesum(sa) * (1.f / D), mb = wavesum(sb) * (1.f / D);
  float qa = 0.f, qb = 0.f;
#pragma unroll
  for (int i = 0; i < 5; ++i) {
    float t = bflo(a[i]) - ma; qa = fmaf(t, t, qa); t = bfhi(a[i]) - ma; qa = fmaf(t, t, qa);
    t = bflo(b[i]) - mb; qb = fmaf(t, t, qb); t = bfhi(b[i]) - mb; qb = fmaf(t, t, qb);
  }
  const float va = wavesum(qa) * (1.f / D), vb = wavesum(qb) * (1.f / D);
  const float ra = __builtin_amdgcn_rcpf(rbf(__builtin_amdgcn_sqrtf(rbf(rbf(va) + 1e-5f))));
  const float rb = __builtin_amdgcn_rcpf(rbf(__builtin_amdgcn_sqrtf(rbf(rbf(vb) + 1e-5f))));
  const float mab = rbf(ma), mbb = rbf(mb);
#pragma unroll
  for (int i = 0; i < 5; ++i) {
    const u32 lo = pk(bflo(a[i]) - mab, bflo(b[i]) - mbb), hi = pk(bfhi(a[i]) - mab, bfhi(b[i]) - mbb);
    LAS u32* o = (LAS u32*)(lds + C_LN + (2 * lane + 128 * i) * 16 + w * 4);
    o[0] = pk(bflo(lo) * ra, bfhi(lo) * rb);
    o[4] = pk(bflo(hi) * ra, bfhi(hi) * rb);
  }
}

DI void aux_publish(const Params& p, LAS unsigned char* lds, int c, int lane) {
  const __amdgpu_buffer_rsrc_t rs = __builtin_amdgcn_make_buffer_rsrc(p.ws, 0, WS_END, 0x00020000);
  constexpr int AX = FUSED ? AUX_SC1 : 0;
#pragma unroll
  for (int i = 0; i < 10; ++i) {
    const v4u v = *(LAS v4u*)(lds + C_LN + i * 1024 + lane * 16);
    __builtin_amdgcn_raw_buffer_store_b128(v, rs, lane * 16, WS_XNT + c * 10240 + i * 1024, AX);
  }
  if (c < 240) {
    const int ks = c / 5, cg = c % 5, g = lane >> 4;
    LAS unsigned char* b = lds + C_W2 + (16 * (g & 1) + 4 * (g >> 1) + ((lane & 15) >> 2)) * 256 + 8 * (lane & 3);
#pragma unroll
    for (int t = 0; t < 8; ++t) {
      const v2u lo = trd(b + t * 32), hi = trd(b + t * 32 + 8 * 256);
      const int T = 8 * cg + t;
      __builtin_amdgcn_raw_buffer_store_b128(v4u{lo.x, lo.y, hi.x, hi.y}, rs, lane * 16, WS_W2T + (((T / 5) * 48 + ks) * 5 + T % 5) * 1024, AX);
    }
  }
}

DI void gather_u(const Params& p, LAS unsigned char* lds, int d0, int lane) {
  const __amdgpu_buffer_rsrc_t rs = __builtin_amdgcn_make_buffer_rsrc(p.ws + WS_XNT, 0, L * D * 2, 0x00020000);
  constexpr int AX = FUSED ? AUX_SC1 : 0;
  v4u v[12];
#pragma unroll
  for (int i = 0; i < 12; ++i) {
    const u32 q = i * 64 + lane;
    v[i] = __builtin_amdgcn_raw_buffer_load_b128(rs, (q / 3) * 10240 + (q % 3) * 16, d0 * 16, AX);
  }
  if (lane < 30) {
#pragma unroll
    for (int ch = 0; ch < 3; ++ch) *(LAS v4u*)(lds + C_U + ch * C_USZ + lane * 16) = v4u{0, 0, 0, 0};
  }
#pragma unroll
  for (int i = 0; i < 12; ++i) {
    const u32 q = i * 64 + lane;
    *(LAS v4u*)(lds + C_U + (q % 3) * C_USZ + 480 + (q / 3) * 16) = v[i];
  }
}

DI f2 cexp(float a2, float br, float lam) {
  const float e = __builtin_amdgcn_exp2f(a2 * lam), ph = __builtin_amdgcn_fractf(br * lam);
  return f2{e * __builtin_amdgcn_cosf(ph), e * __builtin_amdgcn_sinf(ph)};
}

DI void kgen(const Params& p, LAS unsigned char* T, int d, int lane STP) {
  u16 pr[3][6];
#pragma unroll
  for (int ck = 0; ck < 3; ++ck) {
    const int o = d * NS + ck * 64 + lane;
    pr[ck][0] = p.Are[o]; pr[ck][1] = p.Aim[o]; pr[ck][2] = p.Bre[o]; pr[ck][3] = p.Bim[o]; pr[ck][4] = p.Cre[o]; pr[ck][5] = p.Cim[o];
  }
  const float dt = rbf(__builtin_amdgcn_exp2f(bf2f(p.logdt[d]) * 1.44269504f));
  const float dsk = bf2f(p.dskip[d]);
  f16v acc = {};
  LAS u32* tw = (LAS u32*)T + lane;
  LAS unsigned char* tr = T + (lane & 31) * 272 + (lane >> 5) * 16;
#pragma unroll 1
  for (int ck = 0; ck < 3; ++ck) {
    if (ck == 1) STAMP(1);
    u16 q[6];
#pragma unroll
    for (int i = 0; i < 6; ++i) q[i] = ck == 0 ? pr[0][i] : ck == 1 ? pr[1][i] : pr[2][i];
    const float a2 = dt * bf2f(q[0]) * 1.44269504f, br = dt * bf2f(q[1]) * 0.15915494f;
    const float Br = dt * bf2f(q[2]), Bi = dt * bf2f(q[3]), Cr = bf2f(q[4]), Ci = bf2f(q[5]);
    const f2 E1 = cexp(a2, br, 1.f), E8 = cexp(a2, br, 8.f), EH = cexp(a2, br, 256.f);
    const f2 m1 = {E1.x, -E1.y}, m1b = {E1.y, E1.x}, mh = {EH.x, -EH.y}, mhb = {EH.y, EH.x};
    f2 P[8];
    P[0] = f2{Cr * Br - Ci * Bi, -(Cr * Bi + Ci * Br)};
#pragma unroll
    for (int i = 1; i < 8; ++i) P[i] = cmul(P[i - 1], m1, m1b);
#pragma unroll
    for (int i = 0; i < 8; ++i) tw[i * 68] = pk(P[i].x, P[i].y);
    f2 Q[4];
#pragma unroll
    for (int i = 0; i < 4; ++i) { Q[i] = cmul(P[2 * i], mh, mhb); tw[(8 + i) * 68] = pk(Q[i].x, Q[i].y); }
    f2 R0 = cmul(Q[0], mh, mhb), R4 = cmul(Q[2], mh, mhb);
    tw[12 * 68] = pk(R0.x, R0.y); tw[13 * 68] = pk(R4.x, R4.y);
    R0 = cmul(R0, mh, mhb); R4 = cmul(R4, mh, mhb);
    tw[14 * 68] = pk(R0.x, R0.y); tw[15 * 68] = pk(R4.x, R4.y);
#pragma unroll
    for (int i = 0; i < 5; ++i) { R0 = cmul(R0, mh, mhb); tw[(16 + i) * 68] = pk(R0.x, R0.y); }
    const f2 E16 = cmul(E8, E8, f2{-E8.y, E8.x}), E16b = {-E16.y, E16.x};
    f2 Ge = {1.f, 0.f}, Go = E8;
    tw[21 * 68] = 0x00003f80u; tw[22 * 68] = pk(Go.x, Go.y);
#pragma unroll
    for (int i = 1; i < 16; ++i) {
      Ge = i == 1 ? E16 : cmul(Ge, E16, E16b); Go = cmul(Go, E16, E16b);
      tw[(21 + 2 * i) * 68] = pk(Ge.x, Ge.y); tw[(22 + 2 * i) * 68] = pk(Go.x, Go.y);
    }
#pragma unroll
    for (int s = 0; s < 8; ++s) {
      const v4u a = *(LAS v4u*)(tr + s * 32), b = *(LAS v4u*)(tr + 21 * 272 + s * 32);
      acc = mfma32(a, b, acc);
    }
  }
  STAMP(2);
  const int i = lane & 31, hh = lane >> 5;
  if (lane == 0) acc[0] += dsk;
  LAS unsigned char* C = T + T_CMP;
  *(LAS v2u*)(C + (8 * i + 4 * hh) * 2) = v2u{pk(acc[0], acc[1]), pk(acc[2], acc[3])};
  *(LAS u32*)(C + (hh ? 384 + 2 * i : 256 + 4 * i) * 2) = pk(acc[4], acc[5]);
  *(LAS u32*)(C + (hh ? 448 + 2 * i : 258 + 4 * i) * 2) = pk(acc[6], acc[7]);
#pragma unroll
  for (int q = 0; q < 4; ++q) *(LAS u16*)(C + (512 + 128 * hh + 32 * q + i) * 2) = (u16)pk(acc[8 + q], acc[8 + q]);
#pragma unroll
  for (int sg = 0; sg < 4; ++sg) {
    const int r0 = sg == 0 ? 0 : sg == 1 ? 8 : sg == 2 ? 12 : 14, nr = sg == 0 ? 8 : sg == 1 ? 4 : 2, st = sg == 0 ? 16 : sg == 1 ? 32 : sg == 2 ? 64 : 128;
    const int l0 = 2047 - 128 * r0 - 2 * lane;
    const u32 bits = pk((float)l0, (float)(l0 - 1));
    const int i0 = (int)(bits & 0xffffu) - 17024, i1 = (int)(bits >> 16) - 17024;
#pragma unroll
    for (int r = 0; r < nr; ++r) {
      const u32 lo = *(LAS u16*)(C + (i0 - st * r) * 2), hi = *(LAS u16*)(C + (i1 - st * r) * 2);
      *(LAS u32*)(T + T_RA + ((r0 + r) * 64 + lane) * 4) = lo | (hi << 16);
    }
  }
  if (lane < 8) { *(LAS u32*)(T + T_RA + (1024 + lane) * 4) = 0; *(LAS u32*)(T + T_RB + (1024 + lane) * 4) = 0; }
#pragma unroll
  for (int r = 0; r < 4; ++r) {
    const v4u a = *(LAS v4u*)(T + T_RA + (r * 64 + lane) * 16);
    const u32 n = *(LAS u32*)(T + T_RA + (r * 64 + lane) * 16 + 16);
    *(LAS v4u*)(T + T_RB + (r * 64 + lane) * 16) = v4u{__builtin_amdgcn_alignbit(a.y, a.x, 16), __builtin_amdgcn_alignbit(a.z, a.y, 16),
                                                       __builtin_amdgcn_alignbit(a.w, a.z, 16), __builtin_amdgcn_alignbit(n, a.w, 16)};
  }
}

DI void conv(const Params& p, LAS unsigned char* T, LAS unsigned char* U, int d, int lane STP) {
  const int r = lane & 15, g = lane >> 4;
  LAS unsigned char* ap = T + ((r & 1) ? T_RA : T_RB) + ((2031 - r + 8 * g) >> 1) * 4 - 7 * 512;
  LAS unsigned char* bp = U + (224 + 16 * r + 8 * g) * 2;
  f4 acc[8] = {};
#pragma unroll 1
  for (int p0 = 0; p0 < 8; ++p0) {
    v4u a[8], b[8];
#pragma unroll
    for (int i = 0; i < 8; ++i) {
      LAS u32* q = (LAS u32*)(ap + (7 - i) * 512);
      a[i] = v4u{q[0], q[1], q[2], q[3]};
      b[i] = *(LAS v4u*)(bp + i * 512);
    }
#pragma unroll
    for (int s = 0; s < 8; ++s)
#pragma unroll
      for (int o = 0; o <= s; ++o) {
        if (s + o < 8) {
          acc[s + o] = mfma16(a[s], b[o], acc[s + o]);
          if (o != s) acc[s + o] = mfma16(a[o], b[s], acc[s + o]);
        }
      }
    ap -= 64; bp -= 64;
  }
  STAMP(5);
  const __amdgpu_buffer_rsrc_t rs = __builtin_amdgcn_make_buffer_rsrc(p.ws + WS_ST, 0, L * D * 2, 0x00020000);
#pragma unroll
  for (int c = 0; c < 8; c += 2) {
    auto lo = __builtin_amdgcn_permlane16_swap(pk(acc[c][0], acc[c][1]), pk(acc[c + 1][0], acc[c + 1][1]), false, false);
    auto hi = __builtin_amdgcn_permlane16_swap(pk(acc[c][2], acc[c][3]), pk(acc[c + 1][2], acc[c + 1][3]), false, false);
    __builtin_amdgcn_raw_buffer_store_b128(v4u{lo[0], hi[0], lo[1], hi[1]}, rs, (g & 1) * 512 + r * 32 + (g >> 1) * 16 + c * 512, d * 4096, FUSED ? AUX_SC1 : 0);
  }
}

template <int MODE>
__global__ void __launch_bounds__(256, 2) k_main(Params p) {
  extern __shared__ __attribute__((aligned(16))) unsigned char smem[];
  LAS unsigned char* lds = (LAS unsigned char*)smem;
  const int tid = threadIdx.x, b = blockIdx.x;
  STAMP(0);
  if (b < 256) {
    const int m = (b & 7) * 4 + (b >> 6), j = (b >> 3) & 7;
    if (MODE == 1) gemm1(p, lds, m, j, tid STA);
    if (MODE == 3) gemm2(p, lds, m, j, tid STA);
  } else {
    const int c = b - 256, lane = tid & 63, w = __builtin_amdgcn_readfirstlane(tid >> 6);
    if (MODE == 1) {
      if (w == 3 && c < 240) {
        const v4u rs = mkrsrc(p.w2, F * D * 2);
#pragma unroll
        for (int i = 0; i < 8; ++i)
          glds((u32)(size_t)lds + C_W2 + i * 1024, (lane >> 4) * 1280 + (lane & 15) * 16, rs, ((c / 5) * 32 + 4 * i) * 1280 + (c % 5) * 256);
      }
      ln_part(p, lds, c, w, lane);
      STAMP(1);
      WAITV(0);
      __syncthreads();
      STAMP(2);
      if (w == 3) aux_publish(p, lds, c, lane);
    }
    if (MODE == 2) {
      const int d = 3 * c + w;
      if (3 * c < D) {
        if (w == 3) gather_u(p, lds, 3 * c, lane);
        else if (d < D) kgen(p, lds + C_TAB + w * C_TABSZ, d, lane STA);
        STAMP(3);
        __syncthreads();
        STAMP(4);
        if (w < 3 && d < D) conv(p, lds + C_TAB + w * C_TABSZ, lds + C_U + w * C_USZ, d, lane STA);
      }
    }
  }
}

extern "C" void kernel_launch(void* const* d_in, const int* in_sizes, int n_in, void* d_out, int out_size, void* d_ws, size_t ws_size,
                              hipStream_t stream) {
  Params p;
  p.x = (const u16*)d_in[0]; p.Are = (const u16*)d_in[1]; p.Aim = (const u16*)d_in[2]; p.Bre = (const u16*)d_in[3];
  p.Bim = (const u16*)d_in[4]; p.Cre = (const u16*)d_in[5]; p.Cim = (const u16*)d_in[6]; p.logdt = (const u16*)d_in[7];
  p.dskip = (const u16*)d_in[8]; p.w1 = (const u16*)d_in[10]; p.b1 = (const u16*)d_in[11]; p.w2 = (const u16*)d_in[12];
  p.out = (u16*)d_out; p.ws = (unsigned char*)d_ws;
  hipFuncSetAttribute((const void*)k_main<1>, hipFuncAttributeMaxDynamicSharedMemorySize, LDS_BYTES);
  hipFuncSetAttribute((const void*)k_main<2>, hipFuncAttributeMaxDynamicSharedMemorySize, LDS_BYTES);
  hipFuncSetAttribute((const void*)k_main<3>, hipFuncAttributeMaxDynamicSharedMemorySize, LDS_BYTES);
  k_main<1><<<512, 256, LDS_BYTES, stream>>>(p);
  k_main<2><<<512, 256, LDS_BYTES, stream>>>(p);
  k_main<3><<<256, 256, LDS_BYTES, stream>>>(p);
}
```

```cpp
#include <hip/hip_runtime.h>
typedef unsigned short u16;
typedef unsigned u32;
typedef unsigned long long u64;
typedef u32 v4u __attribute__((ext_vector_type(4)));
typedef u32 v2u __attribute__((ext_vector_type(2)));
typedef float f2 __attribute__((ext_vector_type(2)));
typedef float f4 __attribute__((ext_vector_type(4)));
typedef float f16v __attribute__((ext_vector_type(16)));
typedef short s4 __attribute__((ext_vector_type(4)));
typedef __bf16 bf2 __attribute__((ext_vector_type(2)));
typedef __bf16 bf8 __attribute__((ext_vector_type(8)));
typedef _Float16 h2 __attribute__((ext_vector_type(2)));
typedef _Float16 h8 __attribute__((ext_vector_type(8)));
#define LAS __attribute__((address_space(3)))
#define GAS __attribute__((address_space(1)))
#if defined(__HIP_DEVICE_COMPILE__)
#define NOPK __attribute__((target("no-packed-fp32-ops")))
#else
#define NOPK
#endif
#define DI NOPK __device__ __forceinline__

constexpr int L = 2048, D = 640, NS = 192, F = 1536;
constexpr u32 WS_FA = 4096, WS_FG = WS_FA + 8 * 2048, WS_FS = WS_FG + 8192;
constexpr u32 WS_XNT = 65536, WS_W2T = WS_XNT + L * D * 2, WS_ACT = WS_W2T + F * D * 2, WS_ST = WS_ACT + L * F * 2, WS_END = WS_ST + L * D * 2;
constexpr int LDS_BYTES = 80 * 1024;
constexpr int GOFF_G = 0, GOFF_C = 256;
constexpr int SC1 = 16;
constexpr u32 SPIN_CAP = 1u << 16;
#define CODE_KB 16

struct Params {
  const u16 *x, *Are, *Aim, *Bre, *Bim, *Cre, *Cim, *logdt, *dskip, *w1, *b1, *w2;
  u16* out;
  unsigned char* ws;
};
#define STAMP(i)
#define STP
#define STA

typedef __amdgpu_buffer_rsrc_t rsrc_t;
DI float bflo(u32 v) { return __builtin_bit_cast(float, v << 16); }
DI float bfhi(u32 v) { return __builtin_bit_cast(float, v & 0xffff0000u); }
DI float bf2f(u16 v) { return __builtin_bit_cast(float, (u32)v << 16); }
DI u32 pk(float a, float b) { f2 t = {a, b}; return __builtin_bit_cast(u32, __builtin_convertvector(t, bf2)); }
DI float rbf(float a) { return bflo(pk(a, a)); }
DI f4 mfma16(v4u a, v4u b, f4 c) { return __builtin_amdgcn_mfma_f32_16x16x32_bf16(__builtin_bit_cast(bf8, a), __builtin_bit_cast(bf8, b), c, 0, 0, 0); }
DI f16v mfma32(v4u a, v4u b, f16v c) { return __builtin_amdgcn_mfma_f32_32x32x16_bf16(__builtin_bit_cast(bf8, a), __builtin_bit_cast(bf8, b), c, 0, 0, 0); }
DI f16v mfma32h(v4u a, v4u b, f16v c) { return __builtin_amdgcn_mfma_f32_32x32x16_f16(__builtin_bit_cast(h8, a), __builtin_bit_cast(h8, b), c, 0, 0, 0); }
DI f4 mfma4(v2u a, v2u b, f4 c) { return __builtin_amdgcn_mfma_f32_4x4x4bf16_1k(__builtin_bit_cast(s4, a), __builtin_bit_cast(s4, b), c, 0, 0, 0); }
DI v2u trd(LAS unsigned char* p) { return __builtin_bit_cast(v2u, __builtin_amdgcn_ds_read_tr16_b64_v4i16((LAS s4*)p)); }
DI v4u mkrsrc(const void* p, u32 bytes) {
  u64 a = (u64)p;
  return v4u{(u32)a, (u32)(a >> 32) & 0xffffu, bytes, 0x00020000u};
}
DI rsrc_t mkr(const void* p, u32 bytes) { return __builtin_amdgcn_make_buffer_rsrc((void*)p, 0, bytes, 0x00020000); }
DI v4u ld16(rsrc_t r, u32 vo, u32 so) { return __builtin_amdgcn_raw_buffer_load_b128(r, vo, so, SC1); }
DI void st16(v4u v, rsrc_t r, u32 vo, u32 so) { __builtin_amdgcn_raw_buffer_store_b128(v, r, vo, so, SC1); }
DI void setm0(u32 a) { asm volatile("s_mov_b32 m0, %0\n\ts_nop 0" ::"s"(a) : "memory"); }
template <int OFF> DI void glds(u32 voff, v4u rs, u32 soff) {
  asm volatile("buffer_load_dwordx4 %0, %1, %2 offen offset:%3 lds" ::"v"(voff), "s"(rs), "s"(soff), "i"(OFF) : "memory");
}
template <int N> DI void waitv() { asm volatile("s_waitcnt vmcnt(%0)" ::"i"(N) : "memory"); }
#define WAITL0() __builtin_amdgcn_s_waitcnt(0xC07F)
#define BAR() __builtin_amdgcn_s_barrier()
#define SB() __builtin_amdgcn_sched_barrier(0)
DI void syncl() {
  __builtin_amdgcn_fence(__ATOMIC_RELEASE, "workgroup", "local");
  WAITL0();
  BAR();
  __builtin_amdgcn_fence(__ATOMIC_ACQUIRE, "workgroup", "local");
}
template <int CTRL> DI float dpp(float v) { return __builtin_bit_cast(float, __builtin_amdgcn_update_dpp(0, __builtin_bit_cast(int, v), CTRL, 0xf, 0xf, true)); }
DI f2 swap32(float v) {
  u32 u = __builtin_bit_cast(u32, v), u2 = u;
  asm volatile("" : "+v"(u2));
  const v2u r = __builtin_amdgcn_permlane32_swap(u, u2, false, false);
  const u32 a = r.x, b = r.y;
  return f2{__builtin_bit_cast(float, a), __builtin_bit_cast(float, b)};
}
DI f2 swap16(float v) {
  u32 u = __builtin_bit_cast(u32, v), u2 = u;
  asm volatile("" : "+v"(u2));
  const v2u r = __builtin_amdgcn_permlane16_swap(u, u2, false, false);
  const u32 a = r.x, b = r.y;
  return f2{__builtin_bit_cast(float, a), __builtin_bit_cast(float, b)};
}
DI float wavesum(float v) {
  v += dpp<0xB1>(v); v += dpp<0x4E>(v); v += dpp<0x141>(v); v += dpp<0x140>(v);
  f2 t = swap16(v); v = t.x + t.y;
  t = swap32(v); return t.x + t.y;
}
DI float wavemax(float v) {
  v = __builtin_fmaxf(v, dpp<0xB1>(v)); v = __builtin_fmaxf(v, dpp<0x4E>(v)); v = __builtin_fmaxf(v, dpp<0x141>(v)); v = __builtin_fmaxf(v, dpp<0x140>(v));
  f2 t = swap16(v); v = __builtin_fmaxf(t.x, t.y);
  t = swap32(v); return __builtin_fmaxf(t.x, t.y);
}
DI v2u load_epoch(rsrc_t rs) { return __builtin_amdgcn_raw_buffer_load_b64(rs, 0, 0, SC1); }
DI v2u make_tag(v2u e) {
  const u64 E = ((u64)(u32)__builtin_amdgcn_readfirstlane(e.y) << 32) | (u32)__builtin_amdgcn_readfirstlane(e.x);
  const u64 T = E * 6364136223846793005ull + 1442695040888963407ull;
  return v2u{(u32)T, (u32)(T >> 32)};
}
DI bool match2(v4u f, v2u t) { return f.x == t.x && f.y == t.y && f.z == t.x && f.w == t.y; }
DI bool all_(bool b) { return __builtin_amdgcn_ballot_w64(!b) == 0; }
DI void order() { __builtin_amdgcn_fence(__ATOMIC_ACQUIRE, "wavefront"); }
DI void put_flag(rsrc_t rs, u32 vo, u32 so, v2u tag) { __builtin_amdgcn_raw_buffer_store_b64(tag, rs, vo, so, SC1); }
template <int N> DI void wait_flags(rsrc_t rs, u32 vo, u32 so, v2u tag) {
  for (u32 spin = 0; spin < SPIN_CAP; ++spin) {
    bool ok = true;
#pragma unroll
    for (int i = 0; i < N; ++i) ok &= match2(ld16(rs, vo + i * 1024, so), tag);
    if (all_(ok)) break;
    __builtin_amdgcn_s_sleep(2);
  }
  order();
}

constexpr u32 G_W = 0, G_X = 49152, G_CS = 65536, G_B1 = G_CS + 2 * 768, G_SX = G_B1 + 768;
constexpr u32 G_RED = 0, G_S = 61440 + 8192;
static_assert(G_SX + 1024 <= G_S && G_S + 80 * 144 <= LDS_BYTES, "lds");
struct Frags { v4u x[2]; v4u w[2][3]; };
struct G1 {
  f16v acc[3]; f4 sum, sq;
  v4u sel[4];
  u32 xo[4], wo[6], voffX[2], voffW[3];
  v4u rsX, rsW;
  u32 xk, wk, xrow, wcol, ldsw, ldsx;
};
DI void rdx(LAS unsigned char* lds, Frags& f, G1& g, int S4, int k) { f.x[k] = *(LAS v4u*)(lds + g.xo[2 * (S4 & 1) + k] + (S4 >> 1) * 8192); }
DI void rdw(LAS unsigned char* lds, Frags& f, G1& g, int S4, int k, int ft) {
  const v2u a = trd(lds + g.wo[k * 3 + ft] + S4 * 12288), b = trd(lds + g.wo[k * 3 + ft] + S4 * 12288 + 1536);
  f.w[k][ft] = v4u{a.x, a.y, b.x, b.y};
}
DI void sums(Frags& p, G1& g) {
#pragma unroll
  for (int ft = 0; ft < 3; ++ft) g.sum = mfma16(p.w[0][ft], g.sel[ft], g.sum);
  g.sum = mfma16(p.x[0], g.sel[3], g.sum);
  g.sq = mfma4(v2u{p.x[0].x, p.x[0].y}, v2u{p.x[0].x, p.x[0].y}, g.sq);
  g.sq = mfma4(v2u{p.x[0].z, p.x[0].w}, v2u{p.x[0].z, p.x[0].w}, g.sq);
}
#define MM(k, ft) g.acc[ft] = mfma32(prev.w[k][ft], prev.x[k], g.acc[ft])
#define LDW(k, n) wf[(k) % 5][n] = ld16(rs, vo + ((n) & 3) * 1024, wb + (k) * 5120 + ((n) >> 2) * 4096)
#define extra(i) do { if constexpr (EX >= 0) LDW(EX, i); } while (0)
template <int S4, int NV, bool ISSUE, int EX>
DI void g1_stage(LAS unsigned char* lds, Frags& cur, Frags& prev, G1& g, v4u (&wf)[5][5], rsrc_t rs, u32 vo, u32 wb) {
  waitv<NV>();
  WAITL0();
  BAR();
  SB();
  constexpr bool IX = ISSUE && !(S4 & 1);
  if (IX) { setm0(g.ldsx + (((S4 >> 1) + 1) & 1) * 8192); glds<0>(g.voffX[0], g.rsX, g.xrow + g.xk); }
  extra(0);
  rdx(lds, cur, g, S4, 0); rdw(lds, cur, g, S4, 0, 0);
  MM(0, 0); SB();
  if (IX) { glds<1024>(g.voffX[1], g.rsX, g.xrow + g.xk); g.xk += 128; if (g.xk == 1280) g.xk = 0; }
  extra(1);
  rdw(lds, cur, g, S4, 0, 1); rdw(lds, cur, g, S4, 0, 2);
  MM(0, 1); SB();
  if (ISSUE) { setm0(g.ldsw + ((S4 + 3) & 3) * 12288); glds<0>(g.voffW[0], g.rsW, g.wcol + g.wk); }
  extra(2);
  rdx(lds, cur, g, S4, 1); rdw(lds, cur, g, S4, 1, 0);
  MM(0, 2); SB();
  if (ISSUE) glds<1024>(g.voffW[1], g.rsW, g.wcol + g.wk);
  extra(3);
  rdw(lds, cur, g, S4, 1, 1); rdw(lds, cur, g, S4, 1, 2);
  MM(1, 0); SB();
  if (ISSUE) { glds<2048>(g.voffW[2], g.rsW, g.wcol + g.wk); g.wk += 32 * 3072; if (g.wk == 640 * 3072) g.wk = 0; }
  extra(4);
  MM(1, 1); MM(1, 2);
  sums(prev, g);
  SB();
}

DI void g_role(const Params& p, LAS unsigned char* lds, int b, int tid STP) {
  const int m = (b & 7) * 4 + (b >> 6), j = (b >> 3) & 7;
  const int lane = tid & 63, w = __builtin_amdgcn_readfirstlane(tid >> 6), wm = w >> 1, wn = w & 1, fst = wm ^ wn;
  const u32 ldsb = (u32)(size_t)lds;
  G1 g;
  g.rsX = mkrsrc(p.x, L * D * 2); g.rsW = mkrsrc(p.w1, D * F * 2);
  g.voffX[0] = (16 * w + (lane >> 3)) * 1280 + 16 * ((lane & 7) ^ (lane >> 4));
  g.voffX[1] = (16 * w + 8 + (lane >> 3)) * 1280 + 16 * ((lane & 7) ^ (4 + (lane >> 4))) - 1024;
#pragma unroll
  for (int i = 0; i < 3; ++i) {
    const u32 z = i * 64 + lane, r8 = (z * 2731) >> 16, pos = z - r8 * 24;
    g.voffW[i] = (8 * w + r8) * 3072 + ((((pos >> 2) ^ ((r8 >> 1) & 1)) << 2) + (pos & 3)) * 16 - i * 1024;
  }
  const int rot = ((m & 3) * 5 + j * 2 + (j >> 2)) % 10;
  g.xk = rot * 128; g.wk = rot * 64 * 3072;
  g.xrow = m * 64 * 1280; g.wcol = j * 384;
  g.ldsw = ldsb + G_W + w * 3072; g.ldsx = ldsb + G_X + w * 2048;
  STAMP(1);
  setm0(g.ldsx);
  glds<0>(g.voffX[0], g.rsX, g.xrow + g.xk); glds<1024>(g.voffX[1], g.rsX, g.xrow + g.xk);
  g.xk += 128; if (g.xk == 1280) g.xk = 0;
#pragma unroll
  for (int s = 0; s < 3; ++s) {
    setm0(g.ldsw + s * 12288);
    glds<0>(g.voffW[0], g.rsW, g.wcol + g.wk); glds<1024>(g.voffW[1], g.rsW, g.wcol + g.wk); glds<2048>(g.voffW[2], g.rsW, g.wcol + g.wk);
    g.wk += 32 * 3072; if (g.wk == 640 * 3072) g.wk = 0;
  }
  SB();
  const int tok = lane & 31, hh = lane >> 5;
  {
    const u32 row = 32 * wm + tok, bb = G_X + row * 128 + 16 * (hh ^ ((row >> 1) & 7));
    g.xo[0] = bb ^ (32 * fst); g.xo[1] = bb ^ (32 * (1 - fst)); g.xo[2] = g.xo[0] ^ 64; g.xo[3] = g.xo[1] ^ 64;
    const u32 q4 = (lane & 15) >> 2, blk = (lane >> 4) & 1;
#pragma unroll
    for (int ft = 0; ft < 3; ++ft) {
      const u32 a = G_W + (8 * hh + q4) * 384 + 64 * ((3 * wn + ft) ^ (q4 >> 1)) + 32 * blk + 8 * (lane & 3);
      g.wo[ft] = a + 6144 * fst; g.wo[3 + ft] = a + 6144 * (1 - fst);
    }
#pragma unroll
    for (int i = 0; i < 4; ++i) {
      const u32 o = ((lane & 15) == 2 * i + blk) ? 0x3f803f80u : 0u;
      g.sel[i] = v4u{o, o, o, o};
    }
  }
#pragma unroll
  for (int i = 0; i < 3; ++i) g.acc[i] = f16v{};
  g.sum = f4{}; g.sq = f4{};
  Frags A, B;
  B.x[0] = B.x[1] = v4u{0, 0, 0, 0};
#pragma unroll
  for (int k = 0; k < 2; ++k)
#pragma unroll
    for (int ft = 0; ft < 3; ++ft) B.w[k][ft] = v4u{0, 0, 0, 0};
  const rsrc_t rs = mkr(p.ws, WS_END);
  const u32 vo = lane * 16;
  const u32 wb = WS_W2T + (j * 48 + 12 * w) * 5120, ab = WS_ACT + (m * 48 + 12 * w) * 4096;
  v4u wf[5][5];
  STAMP(2);
#pragma unroll 1
  for (int it = 0; it < 4; ++it) {
    g1_stage<0, 6, true, -1>(lds, A, B, g, wf, rs, vo, wb);
    if (it == 0) STAMP(3);
    g1_stage<1, 8, true, -1>(lds, B, A, g, wf, rs, vo, wb);
    g1_stage<2, 6, true, -1>(lds, A, B, g, wf, rs, vo, wb);
    g1_stage<3, 8, true, -1>(lds, B, A, g, wf, rs, vo, wb);
  }
  const v4u rsS = mkrsrc(p.ws, WS_END), rsB = mkrsrc(p.b1 + j * 192, 384);
  v2u ep; v4u fa0, fa1; u32 b1v;
  {
    const u32 so = WS_FA + (b & 7) * 2048;
    asm volatile("buffer_load_dwordx2 %0, off, %5, 0 sc1\n\tbuffer_load_dwordx4 %1, %4, %5, %6 offen sc1\n\t"
                 "buffer_load_dwordx4 %2, %4, %5, %6 offen offset:1024 sc1\n\tbuffer_load_ushort %3, %7, %8, 0 offen"
                 : "=&v"(ep), "=&v"(fa0), "=&v"(fa1), "=&v"(b1v) : "v"(vo), "s"(rsS), "s"(so), "v"(tid * 2), "s"(rsB) : "memory");
  }
  g1_stage<0, 10, true, -1>(lds, A, B, g, wf, rs, vo, wb);
  g1_stage<1, 12, false, -1>(lds, B, A, g, wf, rs, vo, wb);
  asm volatile("s_waitcnt vmcnt(3)" : "+v"(ep), "+v"(fa0), "+v"(fa1), "+v"(b1v)::"memory");
  const v2u tag = make_tag(ep);
  if (!all_(match2(fa0, tag) && match2(fa1, tag))) wait_flags<2>(rs, vo, WS_FA + (b & 7) * 2048, tag);
  order();
  STAMP(4);
  if (tid < 192) *(LAS float*)(lds + G_B1 + tid * 4) = bflo(b1v) * 1.44269504f;
  g1_stage<2, 3, false, 0>(lds, A, B, g, wf, rs, vo, wb);
  g1_stage<3, 5, false, 1>(lds, B, A, g, wf, rs, vo, wb);
  {
    Frags& prev = B;
    sums(prev, g);
    MM(0, 0); MM(1, 0); LDW(2, 0); MM(0, 1); MM(1, 1); LDW(2, 1); MM(0, 2); MM(1, 2);
  }
  {
    const int c = lane & 15, g4 = lane >> 4;
    if (c < 6) *(LAS f4*)(lds + G_CS + wm * 768 + (96 * wn + 16 * c + 4 * g4) * 4) = g.sum;
    else if (c < 8) *(LAS f4*)(lds + G_SX + wn * 512 + (32 * wm + 16 * (c - 6) + 4 * g4) * 4) = g.sum;
    const int q = lane & 3;
    const float dq = q == 0 ? g.sq[0] : q == 1 ? g.sq[1] : q == 2 ? g.sq[2] : g.sq[3];
    const f2 t = swap32(dq);
    if (hh == 0) *(LAS float*)(lds + G_SX + wn * 512 + 256 + (32 * wm + tok) * 4) = t.x + t.y;
  }
  syncl();
  STAMP(5);
  {
    const LAS float* sp = (const LAS float*)(lds + G_SX) + 32 * wm + tok;
    const float mean = (sp[0] + sp[128]) * (1.f / D), var = __builtin_fmaxf((sp[64] + sp[192]) * (1.f / D) - mean * mean, 0.f);
    const float den = rbf(__builtin_amdgcn_sqrtf(rbf(rbf(var) + 1e-5f)));
    const float rinv = __builtin_amdgcn_rcpf(den) * 1.44269504f, nmu = -rbf(mean) * rinv;
    const u32 aoff = ((2 * wm + (tok >> 4)) * 64 + 32 * hh + (tok & 15)) * 16;
#pragma unroll
    for (int ft = 0; ft < 3; ++ft) {
      u32 o[8];
#pragma unroll
      for (int q = 0; q < 4; ++q) {
        const u32 fo = (96 * wn + 32 * ft + 8 * q + 4 * hh) * 4;
        const f4 c0 = *(LAS f4*)(lds + G_CS + fo), c1 = *(LAS f4*)(lds + G_CS + 768 + fo), b4 = *(LAS f4*)(lds + G_B1 + fo);
        float a[4];
#pragma unroll
        for (int e = 0; e < 4; ++e) {
          const float z = __builtin_fmaf(g.acc[ft][4 * q + e], rinv, __builtin_fmaf(c0[e] + c1[e], nmu, b4[e]));
          const float ex = __builtin_amdgcn_exp2f(z);
          const float r = __builtin_amdgcn_rcpf(__builtin_fmaf(ex, __builtin_fmaf(ex, 0.5f, 1.f), 1.f));
          a[e] = __builtin_fmaf(-z, r, z);
        }
        o[2 * q] = pk(a[0], a[1]); o[2 * q + 1] = pk(a[2], a[3]);
        const int li = 12 + (ft * 4 + q) * 2;
        if (li < 25) LDW(li / 5, li % 5);
        if (li + 1 < 25) LDW((li + 1) / 5, (li + 1) % 5);
        SB();
      }
      const u32 so = WS_ACT + (m * 48 + j * 6 + 3 * wn + ft) * 4096;
      st16(v4u{o[0], o[1], o[2], o[3]}, rs, aoff, so);
      st16(v4u{o[4], o[5], o[6], o[7]}, rs, aoff + 256, so);
    }
  }
  STAMP(6);
  waitv<0>();
  if (lane == 0) put_flag(rs, 0, WS_FG + ((m * 8 + j) * 4 + w) * 8, tag);
  STAMP(7);
  wait_flags<1>(rs, (lane & 3) * 16, WS_FG + (m * 8 + 2 * w) * 32, tag);
  STAMP(8);
  v4u af[3][4];
#define LDA(k) _Pragma("unroll") for (int t = 0; t < 4; ++t) af[(k) % 3][t] = ld16(rs, vo + t * 1024, ab + (k) * 4096)
  LDA(0); LDA(1);
  SB();
  f4 acc[4][5] = {};
  v4u sv[3], xv[3];
#pragma unroll
  for (int k = 0; k < 12; ++k) {
    if (k == 1) STAMP(9);
#pragma unroll
    for (int n = 0; n < 5; ++n) {
#pragma unroll
      for (int t = 0; t < 4; ++t) acc[t][n] = mfma16(wf[k % 5][n], af[k % 3][t], acc[t][n]);
      if (n < 4 && k + 2 < 12) af[(k + 2) % 3][n] = ld16(rs, vo + n * 1024, ab + (k + 2) * 4096);
      if (k + 5 < 12) LDW(k + 5, n);
      SB();
    }
    if (k == 8) {
#pragma unroll
      for (int i = 0; i < 3; ++i) {
        const u32 z = i * 64 + lane, r = (z * 6554) >> 16;
        if (z < 160) xv[i] = *(const GAS v4u*)((const GAS unsigned char*)p.x + (size_t)(64 * m + 16 * w + r) * 1280 + j * 160 + (z - r * 10) * 16);
      }
      SB();
    }
    if (k == 9) {
      wait_flags<1>(rs, lane < 40 ? vo : 0, WS_FS + j * 640, tag);
#pragma unroll
      for (int i = 0; i < 3; ++i) {
        const u32 q = i * 256 + tid;
        if (q < 640) sv[i] = ld16(rs, (80 * j + (q >> 3)) * 4096 + (q & 7) * 16, WS_ST + m * 128);
      }
      SB();
    }
  }
  STAMP(10);
  f4 own[5];
#pragma unroll
  for (int t = 0; t < 4; ++t) {
    if (t == w) {
#pragma unroll
      for (int n = 0; n < 5; ++n) own[n] = acc[t][n];
    } else {
      const int src = (w - t - 1) & 3;
#pragma unroll
      for (int n = 0; n < 5; ++n) *(LAS f4*)(lds + G_RED + ((t * 3 + src) * 5 + n) * 1024 + lane * 16) = acc[t][n];
    }
  }
#pragma unroll
  for (int i = 0; i < 3; ++i) {
    const u32 q = i * 256 + tid;
    if (q < 640) *(LAS v4u*)(lds + G_S + (q >> 3) * 144 + (q & 7) * 16) = sv[i];
  }
  syncl();
  STAMP(11);
#pragma unroll
  for (int s = 0; s < 3; ++s)
#pragma unroll
    for (int n = 0; n < 5; ++n) own[n] += *(LAS f4*)(lds + G_RED + ((w * 3 + s) * 5 + n) * 1024 + lane * 16);
  const int g4 = lane >> 4;
  LAS unsigned char* P = lds + G_RED + w * 15360;
  v2u pr[5];
#pragma unroll
  for (int n = 0; n < 5; ++n) {
    const v2u s2 = trd(lds + G_S + (16 * n + 4 * g4 + ((lane & 15) >> 2)) * 144 + (16 * w + 4 * (lane & 3)) * 2);
    float gv[4];
#pragma unroll
    for (int e = 0; e < 4; ++e) {
      const float o = own[n][e];
      gv[e] = (o * 0.69314718f) * __builtin_amdgcn_rcpf(1.f + __builtin_amdgcn_exp2f(-o));
    }
    pr[n] = v2u{pk(bflo(s2.x) * gv[0], bfhi(s2.x) * gv[1]), pk(bflo(s2.y) * gv[2], bfhi(s2.y) * gv[3])};
  }
#pragma unroll
  for (int n = 0; n < 5; ++n) *(LAS v2u*)(P + (lane & 15) * 176 + (16 * n + 4 * g4) * 2) = pr[n];
#pragma unroll
  for (int i = 0; i < 3; ++i) {
    const u32 z = i * 64 + lane, r = (z * 6554) >> 16;
    if (z < 160) {
      const v4u q = *(LAS v4u*)(P + r * 176 + (z - r * 10) * 16);
      v4u o;
#pragma unroll
      for (int e = 0; e < 4; ++e) o[e] = pk(bflo(xv[i][e]) + bflo(q[e]), bfhi(xv[i][e]) + bfhi(q[e]));
      *(GAS v4u*)((GAS unsigned char*)p.out + (size_t)(64 * m + 16 * w + r) * 1280 + j * 160 + (z - r * 10) * 16) = o;
    }
  }
}

constexpr u32 C_LN = 0, C_W2 = 10240, C_TAB = C_W2 + 8192, C_TABSZ = 14464, C_U = C_TAB + 3 * C_TABSZ, C_USZ = 4608, C_END = C_U + 3 * C_USZ;
static_assert(C_END <= LDS_BYTES, "lds");
constexpr u32 T_RA = 0, T_RB = 4160, T_CMP = 8320;

DI void ln_part(const u32 (&a)[5], const u32 (&b)[5], LAS unsigned char* lds, int w, int lane) {
  float sa = 0.f, sb = 0.f;
#pragma unroll
  for (int i = 0; i < 5; ++i) { sa += bflo(a[i]) + bfhi(a[i]); sb += bflo(b[i]) + bfhi(b[i]); }
  const float ma = wavesum(sa) * (1.f / D), mb = wavesum(sb) * (1.f / D);
  float qa = 0.f, qb = 0.f;
#pragma unroll
  for (int i = 0; i < 5; ++i) {
    float t = bflo(a[i]) - ma; qa = __builtin_fmaf(t, t, qa); t = bfhi(a[i]) - ma; qa = __builtin_fmaf(t, t, qa);
    t = bflo(b[i]) - mb; qb = __builtin_fmaf(t, t, qb); t = bfhi(b[i]) - mb; qb = __builtin_fmaf(t, t, qb);
  }
  const float va = wavesum(qa) * (1.f / D), vb = wavesum(qb) * (1.f / D);
  const float ra = __builtin_amdgcn_rcpf(rbf(__builtin_amdgcn_sqrtf(rbf(rbf(va) + 1e-5f))));
  const float rb = __builtin_amdgcn_rcpf(rbf(__builtin_amdgcn_sqrtf(rbf(rbf(vb) + 1e-5f))));
  const float mab = rbf(ma), mbb = rbf(mb);
#pragma unroll
  for (int i = 0; i < 5; ++i) {
    const u32 lo = pk(bflo(a[i]) - mab, bflo(b[i]) - mbb), hi = pk(bfhi(a[i]) - mab, bfhi(b[i]) - mbb);
    LAS u32* o = (LAS u32*)(lds + C_LN + (2 * lane + 128 * i) * 16 + w * 4);
    o[0] = pk(bflo(lo) * ra, bfhi(lo) * rb);
    o[4] = pk(bflo(hi) * ra, bfhi(hi) * rb);
  }
}

DI void aux_publish(rsrc_t rs, LAS unsigned char* lds, int c, int lane) {
#pragma unroll
  for (int i = 0; i < 10; ++i) st16(*(LAS v4u*)(lds + C_LN + i * 1024 + lane * 16), rs, lane * 16, WS_XNT + c * 10240 + i * 1024);
  if (c < 240) {
    const int ks = c / 5, cg = c % 5, g = lane >> 4;
    LAS unsigned char* b = lds + C_W2 + (16 * (g & 1) + 4 * (g >> 1) + ((lane & 15) >> 2)) * 256 + 8 * (lane & 3);
#pragma unroll
    for (int t = 0; t < 8; ++t) {
      const v2u lo = trd(b + t * 32), hi = trd(b + t * 32 + 8 * 256);
      const int T = 8 * cg + t;
      st16(v4u{lo.x, lo.y, hi.x, hi.y}, rs, lane * 16, WS_W2T + (((T / 5) * 48 + ks) * 5 + T % 5) * 1024);
    }
  }
}

DI void gather_u(rsrc_t rs, LAS unsigned char* lds, int d0, int lane) {
  v4u v[12];
  u32 go[3], lo[3];
#pragma unroll
  for (int i = 0; i < 3; ++i) {
    const u32 q = i * 64 + lane, tb = (q * 21846) >> 16, ch = q - 3 * tb;
    go[i] = tb * 10240 + ch * 16; lo[i] = C_U + ch * C_USZ + 480 + tb * 16;
  }
#pragma unroll
  for (int i = 0; i < 12; ++i) v[i] = ld16(rs, go[i % 3], WS_XNT + d0 * 16 + (i / 3) * 64 * 10240);
#pragma unroll
  for (int i = 0; i < 12; ++i) *(LAS v4u*)(lds + lo[i % 3] + (i / 3) * 1024) = v[i];
}

DI f2 cexp(float a2, float br) {
  const float e = __builtin_amdgcn_exp2f(a2), ph = __builtin_amdgcn_fractf(br);
  return f2{e * __builtin_amdgcn_cosf(ph), e * __builtin_amdgcn_sinf(ph)};
}
DI f2 csq(f2 x) { return f2{__builtin_fmaf(x.x, x.x, -(x.y * x.y)), (x.x + x.x) * x.y}; }
DI h2 toh(f2 x) { return __builtin_convertvector(x, h2); }
DI h2 hm(h2 x, h2 m) { const h2 m2 = {-m.y, m.x}; return __builtin_elementwise_fma(x.yy, m2, x.xx * m); }
DI h2 hmc(h2 x, h2 m) { const h2 m1 = {m.x, -m.y}; return __builtin_elementwise_fma(x.yy, m.yx, x.xx * m1); }
DI u32 hb(h2 x) { return __builtin_bit_cast(u32, x); }

DI void kgen(const u16 (&pr)[3][6], float dt, float dsk, LAS unsigned char* T, int lane STP) {
  float a2[3], br[3]; f2 W[3];
  float mx = 0.f;
#pragma unroll
  for (int c = 0; c < 3; ++c) {
    a2[c] = dt * bf2f(pr[c][0]) * 1.44269504f; br[c] = dt * bf2f(pr[c][1]) * 0.15915494f;
    const float Br = bf2f(pr[c][2]), Bi = bf2f(pr[c][3]), Cr = bf2f(pr[c][4]), Ci = bf2f(pr[c][5]);
    W[c] = f2{Cr * Br - Ci * Bi, -(Cr * Bi + Ci * Br)};
    mx = __builtin_fmaxf(mx, __builtin_fmaxf(__builtin_fabsf(W[c].x), __builtin_fabsf(W[c].y)));
  }
  const int ex = __builtin_amdgcn_frexp_expf(wavemax(mx)) - 10;
  h2 Wh[3];
#pragma unroll
  for (int c = 0; c < 3; ++c) Wh[c] = toh(f2{__builtin_amdgcn_ldexpf(W[c].x, -ex), __builtin_amdgcn_ldexpf(W[c].y, -ex)});
  f16v acc = {};
  LAS u32* tw = (LAS u32*)T + lane;
  LAS unsigned char* tr = T + (lane & 31) * 272 + (lane >> 5) * 16;
  v4u fa[8], fb[8];
#pragma unroll
  for (int s = 0; s < 8; ++s) fa[s] = fb[s] = v4u{0, 0, 0, 0};
  tw[21 * 68] = 0x00003c00u;
#pragma unroll 1
  for (int ck = 0; ck < 3; ++ck) {
    if (ck == 1) STAMP(3);
    const f2 E1 = cexp(a2[0], br[0]), E8 = cexp(a2[0] * 8.f, br[0] * 8.f), H1 = cexp(a2[0] * 256.f, br[0] * 256.f);
    const f2 E2 = csq(E1), E16 = csq(E8), H2 = csq(H1), E4 = csq(E2), E32 = csq(E16), H4 = csq(H2), E64 = csq(E32), E128 = csq(E64);
    const h2 e1 = toh(E1), e2 = toh(E2), e4 = toh(E4), h1 = toh(H1), h2_ = toh(H2), h4 = toh(H4);
#pragma unroll
    for (int s = 0; s < 8; ++s) acc = mfma32h(fa[s], fb[s], acc);
    h2 R[21];
    R[0] = Wh[0]; R[1] = hmc(R[0], e1); R[2] = hmc(R[0], e2); R[4] = hmc(R[0], e4);
    R[3] = hmc(R[2], e1); R[5] = hmc(R[4], e1); R[6] = hmc(R[4], e2); R[7] = hmc(R[6], e1);
    R[8] = hmc(R[0], h1); R[9] = hmc(R[2], h1); R[10] = hmc(R[4], h1); R[11] = hmc(R[6], h1);
    R[12] = hmc(R[0], h2_); R[13] = hmc(R[4], h2_); R[14] = hmc(R[12], h1); R[15] = hmc(R[13], h1);
    R[16] = hmc(R[0], h4); R[17] = hmc(R[8], h4); R[18] = hmc(R[12], h4); R[19] = hmc(R[14], h4); R[20] = hmc(R[16], h4);
#pragma unroll
    for (int i = 0; i < 21; ++i) tw[i * 68] = hb(R[i]);
    h2 G[32];
    G[1] = toh(E8); G[2] = toh(E16); G[4] = toh(E32); G[8] = toh(E64); G[16] = toh(E128);
    G[3] = hm(G[2], G[1]); G[12] = hm(G[8], G[4]); G[20] = hm(G[16], G[4]); G[24] = hm(G[16], G[8]); G[28] = hm(G[16], G[12]);
#pragma unroll
    for (int i = 4; i < 32; i += 4)
#pragma unroll
      for (int r = 1; r < 4; ++r) G[i + r] = hm(G[i], G[r]);
#pragma unroll
    for (int i = 1; i < 32; ++i) tw[(21 + i) * 68] = hb(G[i]);
#pragma unroll
    for (int s = 0; s < 8; ++s) { fa[s] = *(LAS v4u*)(tr + s * 32); fb[s] = *(LAS v4u*)(tr + 21 * 272 + s * 32); }
    a2[0] = a2[1]; a2[1] = a2[2]; br[0] = br[1]; br[1] = br[2]; Wh[0] = Wh[1]; Wh[1] = Wh[2];
  }
#pragma unroll
  for (int s = 0; s < 8; ++s) acc = mfma32h(fa[s], fb[s], acc);
  STAMP(4);
  const int i = lane & 31, hh = lane >> 5;
  const float sc = __builtin_amdgcn_ldexpf(dt, ex);
  float k[12];
#pragma unroll
  for (int r = 0; r < 12; ++r) k[r] = acc[r] * sc;
  if (lane == 0) k[0] += dsk;
  LAS unsigned char* C = T + T_CMP;
  *(LAS v2u*)(C + (8 * i + 4 * hh) * 2) = v2u{pk(k[0], k[1]), pk(k[2], k[3])};
  *(LAS u32*)(C + (hh ? 384 + 2 * i : 256 + 4 * i) * 2) = pk(k[4], k[5]);
  *(LAS u32*)(C + (hh ? 448 + 2 * i : 258 + 4 * i) * 2) = pk(k[6], k[7]);
#pragma unroll
  for (int q = 0; q < 4; ++q) *(LAS u16*)(C + (512 + 128 * hh + 32 * q + i) * 2) = (u16)pk(k[8 + q], k[8 + q]);
  u32 e[16];
#pragma unroll
  for (int sg = 0; sg < 4; ++sg) {
    const int r0 = sg == 0 ? 0 : sg == 1 ? 8 : sg == 2 ? 12 : 14, nr = sg == 0 ? 8 : sg == 1 ? 4 : 2, st = sg == 0 ? 16 : sg == 1 ? 32 : sg == 2 ? 64 : 128;
    const int l0 = 2047 - 128 * r0 - 2 * lane;
    const u32 bits = pk((float)l0, (float)(l0 - 1));
    LAS unsigned char* c0 = C + ((int)(bits & 0xffffu) - 17024 - st * (nr - 1)) * 2;
    LAS unsigned char* c1 = C + ((int)(bits >> 16) - 17024 - st * (nr - 1)) * 2;
#pragma unroll
    for (int r = 0; r < nr; ++r) e[r0 + r] = (u32)*(LAS u16*)(c0 + st * (nr - 1 - r) * 2) | ((u32)*(LAS u16*)(c1 + st * (nr - 1 - r) * 2) << 16);
  }
#pragma unroll
  for (int r = 0; r < 16; ++r) *(LAS u32*)(T + T_RA + (r * 64 + lane) * 4) = e[r];
  if (lane < 8) { *(LAS u32*)(T + T_RA + (1024 + lane) * 4) = 0; *(LAS u32*)(T + T_RB + (1024 + lane) * 4) = 0; }
  v4u ra[4]; u32 rn[4];
#pragma unroll
  for (int r = 0; r < 4; ++r) { ra[r] = *(LAS v4u*)(T + T_RA + (r * 64 + lane) * 16); rn[r] = *(LAS u32*)(T + T_RA + (r * 64 + lane) * 16 + 16); }
#pragma unroll
  for (int r = 0; r < 4; ++r)
    *(LAS v4u*)(T + T_RB + (r * 64 + lane) * 16) = v4u{__builtin_amdgcn_alignbit(ra[r].y, ra[r].x, 16), __builtin_amdgcn_alignbit(ra[r].z, ra[r].y, 16),
                                                       __builtin_amdgcn_alignbit(ra[r].w, ra[r].z, 16), __builtin_amdgcn_alignbit(rn[r], ra[r].w, 16)};
}

struct CF { v4u a[8], b[8]; };
DI void conv_rd(CF& f, LAS unsigned char* ap, LAS unsigned char* bp, int i) {
  LAS u32* q = (LAS u32*)(ap + (7 - i) * 512);
  f.a[i] = v4u{q[0], q[1], q[2], q[3]};
  f.b[i] = *(LAS v4u*)(bp + i * 512);
}
DI void conv_step(CF& cur, CF& nxt, f4 (&acc)[8], LAS unsigned char* ap, LAS unsigned char* bp) {
  int n = 0;
#pragma unroll
  for (int i = 0; i < 8; ++i)
#pragma unroll
    for (int o = 0; o + i < 8; ++o) {
      acc[i + o] = mfma16(cur.a[i], cur.b[o], acc[i + o]);
      if ((n & 3) == 0 && n < 32) { conv_rd(nxt, ap, bp, n >> 2); SB(); }
      ++n;
    }
  SB();
}
DI void conv(rsrc_t rs, LAS unsigned char* T, LAS unsigned char* U, int d, int lane STP) {
  const int r = lane & 15, g = lane >> 4;
  LAS unsigned char* ap = T + ((r & 1) ? T_RA : T_RB) + ((2031 - r + 8 * g) >> 1) * 4 - 7 * 512;
  LAS unsigned char* bp = U + (224 + 16 * r + 8 * g) * 2;
  f4 acc[8] = {};
  CF X, Y;
#pragma unroll
  for (int i = 0; i < 8; ++i) conv_rd(X, ap, bp, i);
#pragma unroll 1
  for (int p0 = 0; p0 < 8; p0 += 2) {
    conv_step(X, Y, acc, ap - 64, bp - 64);
    conv_step(Y, X, acc, ap - 128, bp - 128);
    ap -= 128; bp -= 128;
  }
  STAMP(7);
#pragma unroll
  for (int c = 0; c < 8; c += 2) {
    const v2u lo = __builtin_amdgcn_permlane16_swap(pk(acc[c][0], acc[c][1]), pk(acc[c + 1][0], acc[c + 1][1]), false, false);
    const v2u hi = __builtin_amdgcn_permlane16_swap(pk(acc[c][2], acc[c][3]), pk(acc[c + 1][2], acc[c + 1][3]), false, false);
    st16(v4u{lo.x, hi.x, lo.y, hi.y}, rs, (g & 1) * 512 + r * 32 + (g >> 1) * 16 + c * 512, WS_ST + d * 4096);
  }
}

DI void c_role(const Params& p, LAS unsigned char* lds, int c, int tid STP) {
  const int lane = tid & 63, w = __builtin_amdgcn_readfirstlane(tid >> 6), d = 3 * c + w;
  const rsrc_t rs = mkr(p.ws, WS_END);
  const bool chan = w < 3 && d < D;
  const GAS u32* xr = (const GAS u32*)(p.x + (size_t)(8 * c + 2 * w) * D);
  u32 xa[5], xb[5];
#pragma unroll
  for (int i = 0; i < 5; ++i) { xa[i] = xr[lane + 64 * i]; xb[i] = xr[320 + lane + 64 * i]; }
  const v2u ep = load_epoch(rs);
  u16 pr[3][6]; u16 ldt = 0, dsk = 0;
  if (w == 3) {
    if (c < 240) {
      const v4u rw = mkrsrc(p.w2, F * D * 2);
      const u32 so = (c / 5) * 32 * 1280 + (c % 5) * 256, vo = (lane >> 4) * 1280 + (lane & 15) * 16;
#pragma unroll
      for (int i = 0; i < 8; ++i) { setm0((u32)(size_t)lds + C_W2 + i * 1024); glds<0>(vo, rw, so + i * 4 * 1280); }
    }
    if (lane < 30) {
#pragma unroll
      for (int ch = 0; ch < 3; ++ch) *(LAS v4u*)(lds + C_U + ch * C_USZ + lane * 16) = v4u{0, 0, 0, 0};
    }
  } else {
    const int dd = chan ? d : 0;
#pragma unroll
    for (int ck = 0; ck < 3; ++ck) {
      const int o = dd * NS + ck * 64 + lane;
      pr[ck][0] = p.Are[o]; pr[ck][1] = p.Aim[o]; pr[ck][2] = p.Bre[o]; pr[ck][3] = p.Bim[o]; pr[ck][4] = p.Cre[o]; pr[ck][5] = p.Cim[o];
    }
    ldt = p.logdt[dd]; dsk = p.dskip[dd];
  }
  SB();
  STAMP(1);
  ln_part(xa, xb, lds, w, lane);
  syncl();
  STAMP(2);
  const v2u tag = make_tag(ep);
  if (w == 3) {
    waitv<0>();
    aux_publish(rs, lds, c, lane);
    STAMP(3);
    waitv<0>();
    if (lane < 8) put_flag(rs, lane * 2048, WS_FA + c * 8, tag);
    STAMP(4);
    if (3 * c < D) {
      wait_flags<2>(rs, lane * 16, WS_FA + (c & 7) * 2048, tag);
      STAMP(5);
      gather_u(rs, lds, 3 * c, lane);
      syncl();
    }
  } else if (chan) {
    LAS unsigned char* T = lds + C_TAB + w * C_TABSZ;
    kgen(pr, rbf(__builtin_amdgcn_exp2f(bf2f(ldt) * 1.44269504f)), bf2f(dsk), T, lane STA);
    STAMP(5);
    syncl();
    STAMP(6);
    conv(rs, T, lds + C_U + w * C_USZ, d, lane STA);
    waitv<0>();
    if (lane == 0) put_flag(rs, 0, WS_FS + d * 8, tag);
  } else if (c == 255 && w == 0) {
    wait_flags<2>(rs, lane * 16, WS_FA, tag);
    wait_flags<8>(rs, lane * 16, WS_FG, tag);
    wait_flags<5>(rs, lane * 16, WS_FS, tag);
    if (lane == 0) put_flag(rs, 0, 0, tag);
  }
}

NOPK __global__ void __launch_bounds__(256, 2) k_main(Params p) {
  extern __shared__ __attribute__((aligned(16))) unsigned char smem[];
  LAS unsigned char* lds = (LAS unsigned char*)smem;
  const int tid = __builtin_amdgcn_workitem_id_x(), b = __builtin_amdgcn_workgroup_id_x();
  STAMP(0);
  if (b < 256) g_role(p, lds, b, tid STA);
  else c_role(p, lds, b - 256, tid STA);
}

extern "C" void kernel_launch(void* const* d_in, const int* in_sizes, int n_in, void* d_out, int out_size, void* d_ws, size_t ws_size,
                              hipStream_t stream) {
  Params p;
  p.x = (const u16*)d_in[0]; p.Are = (const u16*)d_in[1]; p.Aim = (const u16*)d_in[2]; p.Bre = (const u16*)d_in[3];
  p.Bim = (const u16*)d_in[4]; p.Cre = (const u16*)d_in[5]; p.Cim = (const u16*)d_in[6]; p.logdt = (const u16*)d_in[7];
  p.dskip = (const u16*)d_in[8]; p.w1 = (const u16*)d_in[10]; p.b1 = (const u16*)d_in[11]; p.w2 = (const u16*)d_in[12];
  p.out = (u16*)d_out; p.ws = (unsigned char*)d_ws;
  (void)hipFuncSetAttribute((const void*)k_main, hipFuncAttributeMaxDynamicSharedMemorySize, LDS_BYTES);
  k_main<<<512, 256, LDS_BYTES, stream>>>(p);
}
```

```cpp
#include <hip/hip_runtime.h>
typedef unsigned short u16;
typedef unsigned u32;
typedef unsigned long long u64;
typedef u32 v4u __attribute__((ext_vector_type(4)));
typedef u32 v2u __attribute__((ext_vector_type(2)));
typedef float f2 __attribute__((ext_vector_type(2)));
typedef float f4 __attribute__((ext_vector_type(4)));
typedef float f16v __attribute__((ext_vector_type(16)));
typedef short s4 __attribute__((ext_vector_type(4)));
typedef __bf16 bf2 __attribute__((ext_vector_type(2)));
typedef __bf16 bf8 __attribute__((ext_vector_type(8)));
typedef _Float16 h2 __attribute__((ext_vector_type(2)));
typedef _Float16 h8 __attribute__((ext_vector_type(8)));
#define LAS __attribute__((address_space(3)))
#define GAS __attribute__((address_space(1)))
#if defined(__HIP_DEVICE_COMPILE__)
#define NOPK __attribute__((target("no-packed-fp32-ops")))
#else
#define NOPK
#endif
#define DI NOPK __device__ __forceinline__

constexpr int L = 2048, D = 640, NS = 192, F = 1536;
constexpr u32 WS_FA = 4096, WS_GD = 32768, WS_FW = 49152, WS_FS = WS_FW + 32768, WS_FG = WS_FS + 98304, WS_FG2 = WS_FG + 131072;
constexpr u32 WS_STAT = WS_FG2 + 131072, WS_CSP = WS_STAT + 8192;
static_assert(WS_CSP + 61440 + 1024 <= 589824, "ws");
constexpr u32 WS_XNT = 589824, WS_W2T = WS_XNT + L * D * 2, WS_ACT = WS_W2T + F * D * 2, WS_ST = WS_ACT + L * F * 2, WS_END = WS_ST + L * D * 2;
constexpr int LDS_BYTES = 80 * 1024;
constexpr int SC1 = 16;
constexpr u32 SPIN_CAP = 1u << 16;

struct Params {
  const u16 *x, *Are, *Aim, *Bre, *Bim, *Cre, *Cim, *logdt, *dskip, *w1, *b1, *w2;
  u16* out;
  unsigned char* ws;
};

typedef __amdgpu_buffer_rsrc_t rsrc_t;
DI float bflo(u32 v) { return __builtin_bit_cast(float, v << 16); }
DI float bfhi(u32 v) { return __builtin_bit_cast(float, v & 0xffff0000u); }
DI float bf2f(u16 v) { return __builtin_bit_cast(float, (u32)v << 16); }
DI u32 pk(float a, float b) { f2 t = {a, b}; return __builtin_bit_cast(u32, __builtin_convertvector(t, bf2)); }
DI float rbf(float a) { return bflo(pk(a, a)); }
DI f4 mfma16(v4u a, v4u b, f4 c) { return __builtin_amdgcn_mfma_f32_16x16x32_bf16(__builtin_bit_cast(bf8, a), __builtin_bit_cast(bf8, b), c, 0, 0, 0); }
DI f16v mfma32(v4u a, v4u b, f16v c) { return __builtin_amdgcn_mfma_f32_32x32x16_bf16(__builtin_bit_cast(bf8, a), __builtin_bit_cast(bf8, b), c, 0, 0, 0); }
DI f16v mfma32h(v4u a, v4u b, f16v c) { return __builtin_amdgcn_mfma_f32_32x32x16_f16(__builtin_bit_cast(h8, a), __builtin_bit_cast(h8, b), c, 0, 0, 0); }
DI v2u trd(LAS unsigned char* p) { return __builtin_bit_cast(v2u, __builtin_amdgcn_ds_read_tr16_b64_v4i16((LAS s4*)p)); }
DI v4u mkrsrc(const void* p, u32 bytes) {
  u64 a = (u64)p;
  return v4u{(u32)a, (u32)(a >> 32) & 0xffffu, bytes, 0x00020000u};
}
DI rsrc_t mkr(const void* p, u32 bytes) { return __builtin_amdgcn_make_buffer_rsrc((void*)p, 0, bytes, 0x00020000); }
DI v4u ld16(rsrc_t r, u32 vo, u32 so) { return __builtin_amdgcn_raw_buffer_load_b128(r, vo, so, SC1); }
template <int AUX = SC1> DI void st16(v4u v, rsrc_t r, u32 vo, u32 so) {
  __builtin_amdgcn_raw_buffer_store_b128(v, r, vo, so, AUX);
  asm volatile("s_nop 1" ::"v"(v) : "memory");
}
DI void setm0(u32 a) { asm volatile("s_mov_b32 m0, %0\n\ts_nop 0" ::"s"(a) : "memory"); }
template <int OFF> DI void glds(u32 voff, v4u rs, u32 soff) {
  asm volatile("buffer_load_dwordx4 %0, %1, %2 offen offset:%3 lds" ::"v"(voff), "s"(rs), "s"(soff), "i"(OFF) : "memory");
}
template <int N> DI void waitv() { asm volatile("s_waitcnt vmcnt(%0)" ::"i"(N) : "memory"); }
#define WAITL0() __builtin_amdgcn_s_waitcnt(0xC07F)
#define BAR() __builtin_amdgcn_s_barrier()
#define SB() __builtin_amdgcn_sched_barrier(0)
DI void syncl() {
  __builtin_amdgcn_fence(__ATOMIC_RELEASE, "workgroup", "local");
  WAITL0();
  BAR();
  __builtin_amdgcn_fence(__ATOMIC_ACQUIRE, "workgroup", "local");
}
template <int CTRL> DI float dpp(float v) { return __builtin_bit_cast(float, __builtin_amdgcn_update_dpp(0, __builtin_bit_cast(int, v), CTRL, 0xf, 0xf, true)); }
DI f2 swap16(float v) {
  u32 u = __builtin_bit_cast(u32, v), u2 = u;
  asm volatile("" : "+v"(u2));
  const v2u r = __builtin_amdgcn_permlane16_swap(u, u2, false, false);
  const u32 a = r.x, b = r.y;
  return f2{__builtin_bit_cast(float, a), __builtin_bit_cast(float, b)};
}
template <int CTRL> DI u32 dppmax(u32 v) { const u32 o = __builtin_amdgcn_update_dpp(0, (int)v, CTRL, 0xf, 0xf, true); return v > o ? v : o; }
DI float wavemax(float x) {
  u32 v = __builtin_bit_cast(u32, x);
  v = dppmax<0xB1>(v); v = dppmax<0x4E>(v); v = dppmax<0x141>(v); v = dppmax<0x140>(v);
  const u32 a = __builtin_amdgcn_readlane(v, 0), b = __builtin_amdgcn_readlane(v, 16), c = __builtin_amdgcn_readlane(v, 32), d = __builtin_amdgcn_readlane(v, 48);
  const u32 e = a > b ? a : b, f = c > d ? c : d;
  return __builtin_bit_cast(float, e > f ? e : f);
}
DI v2u make_tag(v2u e) {
  const u64 E = ((u64)(u32)__builtin_amdgcn_readfirstlane(e.y) << 32) | (u32)__builtin_amdgcn_readfirstlane(e.x);
  const u64 T = E * 6364136223846793005ull + 1442695040888963407ull;
  return v2u{(u32)T, (u32)(T >> 32)};
}
DI bool match2(v4u f, v2u t) { return f.x == t.x && f.y == t.y && f.z == t.x && f.w == t.y; }
DI bool all_(bool b) { return __builtin_amdgcn_ballot_w64(!b) == 0; }
DI int relane(u32 vo) { asm volatile("" : "+v"(vo)); return vo >> 4; }
template <class T> DI void pinv(T& x) { asm volatile("" : "+v"(x)); }
template <class T> DI void pins(T& x) { asm volatile("" : "+s"(x)); }
DI void order() { __builtin_amdgcn_fence(__ATOMIC_ACQUIRE, "wavefront"); }
DI void put_flag(rsrc_t rs, u32 vo, u32 so, v2u tag) { __builtin_amdgcn_raw_buffer_store_b64(tag, rs, vo, so, SC1); }
DI void put_flag2(rsrc_t rs, u32 vo, u32 so, v2u tag) { st16(v4u{tag.x, tag.y, tag.x, tag.y}, rs, vo, so); }
template <int N, int NAP = 8, int STEP = 1024> DI void wait_flags(rsrc_t rs, u32 vo, u32 so, v2u tag) {
#pragma unroll 1
  for (u32 spin = 0; spin < SPIN_CAP; ++spin) {
    bool ok = true;
#pragma unroll
    for (int i = 0; i < N; ++i) ok &= match2(ld16(rs, vo + i * STEP, so), tag);
    if (all_(ok)) break;
    __builtin_amdgcn_s_sleep(NAP);
    asm volatile("" ::: "memory");
  }
  order();
}

constexpr u32 G_AUG = 0;
constexpr u32 G_S = 61440, G_XT = G_S + 80 * 128;
static_assert(G_XT + 64 * 160 <= LDS_BYTES, "lds");
struct G1 {
  f16v acc[3];
  v4u x[2][4], wa[4], wb[4], wc[4];
  u32 xo[4], wo, vx[2], vw[2];
  v4u rsX, rsW;
  u32 xk, wk, rpos, ipos, iend;
  u32 wp, xp[4];
};
DI v4u rdw(LAS unsigned char* p, int off) {
  const v2u a = trd(p + off), b = trd(p + off + 512);
  return v4u{a.x, a.y, b.x, b.y};
}
template <int I> DI void prem0(G1& g) { asm volatile("s_mov_b32 m0, %0" ::"s"(g.ipos + I * 4096) : "memory"); }
template <int ODD, int I> DI void issue1(G1& g) {
  if (!ODD && I < 2) glds<(I & 1) * 1024>(g.vx[I & 1], g.rsX, g.xk);
  else glds<(I & 1) * 1024>(g.vw[I & 1], g.rsW, g.wk + (ODD ? 128 + (I >> 1) * 128 : 0));
}
template <int ODD> DI void advance(G1& g) {
  g.ipos += 16384; if (g.ipos == g.iend) g.ipos -= 81920;
  if (ODD) { g.xk += 128; g.wk += 64 * 3072; }
}
template <int ODD> DI void issue4(G1& g) {
  setm0(g.ipos); issue1<ODD, 0>(g); issue1<ODD, 1>(g); setm0(g.ipos + 8192); issue1<ODD, 2>(g); issue1<ODD, 3>(g); advance<ODD>(g);
}
#define kprod(k) ((k) < 8 ? (k) >> 2 : ((k) - 8) >> 1)
#define kstep(k) ((k) < 8 ? (k) & 3 : 4 + ((k) & 1))
#define FGO (vo & 0x380)
#define FSO (((vo << 2) & 0x380) + ((w < 2 ? (vo >= 768 ? 512 : vo & 768) : vo & 256) << 4))
#define LDW(k, n) wf[k][n] = ld16(rs, vo, (kprod(k) ? wb1 : wb0) + kstep(k) * 5120 + (n) * 1024)
#define SIDE(i) do { if (ISSUE) issue1<ODD, i>(g); if constexpr (EX >= 0 && ((i) & 1)) { LDW((EX + (i) / 2) / 5, (EX + (i) / 2) % 5); } } while (0)
#define MM(t, W, X, k) g.acc[t] = mfma32(W[k], X[k], g.acc[t])
#define M0(i) do { if (ISSUE) prem0<i>(g); } while (0)
template <int ODD, int P, int NV, bool ISSUE, int EX>
DI void g1_stage(LAS unsigned char* lds, G1& g, v4u (&wf)[12][5], rsrc_t rs, u32 vo, u32 wb0, u32 wb1) {
  LAS unsigned char* wp = (LAS unsigned char*)(size_t)g.wp;
  waitv<NV>();
  WAITL0();
  BAR();
  SB();
  if (!ODD) {
    MM(1, g.wb, g.x[P ^ 1], 0); SB();
    M0(0); g.x[P][0] = *(LAS v4u*)(size_t)g.xp[0]; g.x[P][1] = *(LAS v4u*)(size_t)g.xp[1]; g.wa[0] = rdw(wp, 8192); SB();
    MM(2, g.wc, g.x[P ^ 1], 0); SB();
    SIDE(0); g.x[P][2] = *(LAS v4u*)(size_t)g.xp[2]; g.x[P][3] = *(LAS v4u*)(size_t)g.xp[3]; g.wa[1] = rdw(wp, 8192 + 2048); SB();
    MM(1, g.wb, g.x[P ^ 1], 1); SB();
    SIDE(1); M0(2); g.wa[2] = rdw(wp, 8192 + 4096); g.wa[3] = rdw(wp, 8192 + 6144); SB();
    MM(2, g.wc, g.x[P ^ 1], 1); SB();
    SIDE(2);
    g.rpos += 16384; if (g.rpos == 81920) g.rpos = 0;
    g.wp = g.wo + g.rpos; pinv(g.wp); SB();
    MM(1, g.wb, g.x[P ^ 1], 2); SB();
    SIDE(3); if (ISSUE) { advance<ODD>(g); pins(g.ipos); } SB();
    MM(2, g.wc, g.x[P ^ 1], 2); SB();
  } else {
    MM(1, g.wb, g.x[P ^ 1], 3); SB();
    M0(0); g.wb[0] = rdw(wp, 0); g.wc[0] = rdw(wp, 8192); SB();
    MM(2, g.wc, g.x[P ^ 1], 3); SB();
    SIDE(0); g.wb[1] = rdw(wp, 2048); g.wc[1] = rdw(wp, 8192 + 2048); SB();
    MM(0, g.wa, g.x[P], 0); SB();
    SIDE(1); M0(2); g.wb[2] = rdw(wp, 4096); g.wc[2] = rdw(wp, 8192 + 4096); SB();
    MM(0, g.wa, g.x[P], 1); SB();
    SIDE(2); g.wb[3] = rdw(wp, 6144); g.wc[3] = rdw(wp, 8192 + 6144);
    g.rpos += 16384; if (g.rpos == 81920) g.rpos = 0;
    pins(g.rpos); SB();
    MM(0, g.wa, g.x[P], 2); SB();
    SIDE(3);
    g.wp = g.wo + g.rpos; pinv(g.wp);
#pragma unroll
    for (int k = 0; k < 4; ++k) { g.xp[k] = g.xo[k] + g.rpos; pinv(g.xp[k]); }
    SB();
    MM(0, g.wa, g.x[P], 3); SB();
    if (ISSUE) { advance<ODD>(g); pins(g.ipos); pins(g.xk); pins(g.wk); }
    SB();
  }
}

DI void mm4(f4& c0, f4& c1, f4& c2, f4& c3, v4u a, const v4u (&b)[4]) {
  asm("v_mfma_f32_16x16x32_bf16 %0, %4, %5, %0\n\tv_mfma_f32_16x16x32_bf16 %1, %4, %6, %1\n\t"
      "v_mfma_f32_16x16x32_bf16 %2, %4, %7, %2\n\tv_mfma_f32_16x16x32_bf16 %3, %4, %8, %3"
      : "+v"(c0), "+v"(c1), "+v"(c2), "+v"(c3) : "v"(a), "v"(b[0]), "v"(b[1]), "v"(b[2]), "v"(b[3]));
}
DI void mm4z(f4& c0, f4& c1, f4& c2, f4& c3, v4u a, const v4u (&b)[4]) {
  asm("v_mfma_f32_16x16x32_bf16 %0, %4, %5, 0\n\tv_mfma_f32_16x16x32_bf16 %1, %4, %6, 0\n\t"
      "v_mfma_f32_16x16x32_bf16 %2, %4, %7, 0\n\tv_mfma_f32_16x16x32_bf16 %3, %4, %8, 0"
      : "=&v"(c0), "=&v"(c1), "=&v"(c2), "=&v"(c3) : "v"(a), "v"(b[0]), "v"(b[1]), "v"(b[2]), "v"(b[3]));
}
DI void mm5(f4 (&c)[5], const v4u (&a)[5], v4u b) {
  asm("v_mfma_f32_16x16x32_bf16 %0, %5, %10, %0\n\tv_mfma_f32_16x16x32_bf16 %1, %6, %10, %1\n\tv_mfma_f32_16x16x32_bf16 %2, %7, %10, %2\n\t"
      "v_mfma_f32_16x16x32_bf16 %3, %8, %10, %3\n\tv_mfma_f32_16x16x32_bf16 %4, %9, %10, %4"
      : "+v"(c[0]), "+v"(c[1]), "+v"(c[2]), "+v"(c[3]), "+v"(c[4]) : "v"(a[0]), "v"(a[1]), "v"(a[2]), "v"(a[3]), "v"(a[4]), "v"(b));
}
DI void mm5w(f4 (&c)[5], const v4u (&a)[5], v4u b, const f4 (&o)[5], u32 addr) {
  asm volatile("v_mfma_f32_16x16x32_bf16 %0, %5, %10, %0\n\tds_write_b128 %16, %11\n\tv_mfma_f32_16x16x32_bf16 %1, %6, %10, %1\n\tds_write_b128 %16, %12 offset:1024\n\t"
      "v_mfma_f32_16x16x32_bf16 %2, %7, %10, %2\n\tds_write_b128 %16, %13 offset:2048\n\tv_mfma_f32_16x16x32_bf16 %3, %8, %10, %3\n\tds_write_b128 %16, %14 offset:3072\n\t"
      "v_mfma_f32_16x16x32_bf16 %4, %9, %10, %4\n\tds_write_b128 %16, %15 offset:4096"
      : "+v"(c[0]), "+v"(c[1]), "+v"(c[2]), "+v"(c[3]), "+v"(c[4])
      : "v"(a[0]), "v"(a[1]), "v"(a[2]), "v"(a[3]), "v"(a[4]), "v"(b), "v"(o[0]), "v"(o[1]), "v"(o[2]), "v"(o[3]), "v"(o[4]), "v"(addr) : "memory");
}

DI void g_role(const Params& p, LAS unsigned char* lds, int b, int tid) {
  const int m = ((b & 7) >> 1) * 8 + (b >> 5), j = (b & 1) * 4 + ((b >> 3) & 3);
  const int lane = tid & 63, w = __builtin_amdgcn_readfirstlane(tid >> 6), wm = w >> 1, wn = w & 1;
  const u32 ldsb = (u32)(size_t)lds;
  __builtin_amdgcn_s_setprio(2);
  G1 g;
  g.vx[0] = (16 * w + (lane >> 3)) * 1280 + 16 * ((lane & 7) ^ (lane >> 4));
  g.vx[1] = (g.vx[0] ^ 64) + 8 * 1280 - 1024;
  g.vw[0] = (16 * w + (lane >> 3)) * 3072 + 16 * ((lane & 7) ^ ((lane >> 2) & 4));
  g.vw[1] = g.vw[0] + 8 * 3072 - 1024;
  pinv(g.vx[0]); pinv(g.vx[1]); pinv(g.vw[0]); pinv(g.vw[1]);
  SB();
  g.rsX = mkrsrc(p.x, L * D * 2); g.rsW = mkrsrc(p.w1, D * F * 2);
  const int par = (m + j) & 1;
  g.xk = m * 64 * 1280; g.wk = j * 384;
  g.ipos = ldsb + w * 2048; g.iend = g.ipos + 81920; g.rpos = 0;
  const rsrc_t rs = mkr(p.ws, WS_END);
  const v4u rsS = mkrsrc(p.ws, WS_END);
  v2u ep;
  pins(g.rsX); pins(g.rsW);
  __builtin_amdgcn_s_sleep(14);
  issue4<0>(g);
  issue4<1>(g);
  SB();
  const int tok = lane & 31, hh = lane >> 5;
  {
    const u32 row = 32 * wm + tok, bb = row * 128 + 16 * (hh ^ ((row >> 1) & 7));
    g.xo[0] = ldsb + bb; g.xo[1] = ldsb + (bb ^ 32); g.xo[2] = ldsb + (bb ^ 64); g.xo[3] = ldsb + (bb ^ 96);
    const u32 q4 = (lane & 15) >> 2, blk = (lane >> 4) & 1;
    g.wo = ldsb + (8 * hh + q4) * 128 + 64 * (wn ^ (q4 >> 1)) + 32 * blk + 8 * (lane & 3);
    g.wp = g.wo;
#pragma unroll
    for (int k = 0; k < 4; ++k) g.xp[k] = g.xo[k];
  }
#pragma unroll
  for (int i = 0; i < 3; ++i) g.acc[i] = f16v{};
#pragma unroll
  for (int k = 0; k < 4; ++k) g.x[1][k] = g.wb[k] = g.wc[k] = v4u{0, 0, 0, 0};
#pragma unroll
  for (int k = 0; k < 4; ++k) { pinv(g.x[1][k]); pinv(g.wb[k]); pinv(g.wc[k]); pinv(g.xo[k]); pinv(g.xp[k]); }
  pinv(g.acc[0]); pinv(g.acc[1]); pinv(g.acc[2]); pinv(g.wo); pinv(g.wp);
  SB();
  issue4<0>(g); issue4<1>(g);
  SB();
  const u32 vo = lane * 16;
  const u32 wb0 = WS_W2T + (j * 48 + 12 * w + 6 * par) * 5120, wb1 = WS_W2T + (j * 48 + 12 * w + 6 - 6 * par) * 5120;
  v4u wf[12][5];
  const v4u rsB = mkrsrc(p.b1 + j * 192, 384);
  v4u fa0; u32 b1v;
#define GA lds, g, wf, rs, vo, wb0, wb1
#pragma unroll 1
  for (int it = 0; it < 3; ++it) {
    g1_stage<0, 0, 12, true, -1>(GA);
    g1_stage<1, 0, 12, true, -1>(GA);
    g1_stage<0, 1, 12, true, -1>(GA);
    g1_stage<1, 1, 12, true, -1>(GA);
  }
  g1_stage<0, 0, 12, true, -1>(GA);
  u32 fwo;
  {
    const u32 ks = 12 * w + (lane >> 1), k6 = (ks * 43) >> 8, cg = (5 * j + 4 * (lane & 1)) >> 3;
    const u32 sp = 3 * (j & 3) + (w < 3 ? w : 0), kc = lane - 24 < 10 ? lane - 24 : 0;
    fwo = (lane < 24 ? 32 * k6 + 5 * (ks - 6 * k6) + cg : lane < 40 ? 32 * (2 * (j & 3) + (j >> 2)) + 10 * (sp - 3 * (j & 3)) + kc : 8 * m + 4 * wm + (lane & 3)) * 128;
  }
  asm volatile("buffer_load_dwordx4 %0, %3, %4, %5 offen sc1\n\tbuffer_load_ushort %1, %6, %7, 0 offen\n\tbuffer_load_dwordx2 %2, off, %4, 0 sc1"
               : "=&v"(fa0), "=&v"(b1v), "=&v"(ep) : "v"(fwo), "s"(rsS), "s"(WS_FW), "v"(tid * 2), "s"(rsB) : "memory");
  g1_stage<1, 0, 15, true, -1>(GA);
  g1_stage<0, 1, 15, true, -1>(GA);
  g1_stage<1, 1, 15, true, -1>(GA);
  asm volatile("s_waitcnt vmcnt(12)" : "+v"(fa0), "+v"(b1v), "+v"(ep)::"memory");
  const v2u tag = make_tag(ep);
  if (!all_(match2(fa0, tag))) wait_flags<1, 4>(rs, fwo, WS_FW, tag);
  order();
  float csp[10];
#pragma unroll
  for (int i = 0; i < 10; ++i) csp[i] = __builtin_bit_cast(float, __builtin_amdgcn_raw_buffer_load_b32(rs, tid * 4, WS_CSP + i * 6144 + j * 768, SC1));
  const u32 stat = __builtin_amdgcn_raw_buffer_load_b32(rs, (32 * wm + tok) * 4, WS_STAT + m * 256, SC1);
  SB();
  g1_stage<0, 0, 63, false, 0>(GA);
  g1_stage<1, 0, 8 + 11 + 2, false, 2>(GA);
  g1_stage<0, 1, 4 + 11 + 4, false, 4>(GA);
  {
    const float cs = ((csp[0] + csp[1]) + (csp[2] + csp[3])) + ((csp[4] + csp[5]) + (csp[6] + csp[7])) + (csp[8] + csp[9]);
    const float c1 = rbf(cs), c2 = rbf(cs - c1);
    *(LAS v2u*)(lds + G_AUG + tid * 8) = v2u{pk(c1, c2), (pk(cs - c1 - c2, 0.f) & 0xffffu) | (b1v << 16)};
  }
  g1_stage<1, 1, 11 + 6, false, 6>(GA);
  v2u aug[3];
#pragma unroll
  for (int t = 0; t < 3; ++t) aug[t] = *(LAS v2u*)(lds + G_AUG + (64 * t + 32 * wn + tok) * 8);
  {
    const u32 nm = (stat & 0xffffu) ^ 0x8000u;
    const v4u xa = hh ? v4u{0, 0, 0, 0} : v4u{nm | (nm << 16), nm | (stat & 0xffff0000u), 0, 0};
#pragma unroll
    for (int t = 0; t < 3; ++t) g.acc[t] = mfma32(v4u{aug[t].x, aug[t].y, 0, 0}, xa, g.acc[t]);
    SB(); LDW(1, 3); LDW(1, 4); SB();
    MM(1, g.wb, g.x[1], 0); SB();
  }
  {
    const float rinv = __builtin_amdgcn_rcpf(bfhi(stat)) * 1.44269504f;
    const u32 aoff = ((2 * wm + (tok >> 4)) * 64 + 32 * hh + (tok & 15)) * 16;
#pragma unroll
    for (int ft = 0; ft < 3; ++ft) {
      u32 o[8];
#pragma unroll
      for (int q = 0; q < 2; ++q) {
        float z[8], ex[8], r[8];
        const int li = 12 + (2 * ft + q) * 3;
#pragma unroll
        for (int e = 0; e < 8; ++e) { z[e] = g.acc[ft][8 * q + e] * rinv; ex[e] = __builtin_amdgcn_exp2f(z[e]); }
        SB(); LDW(li / 5, li % 5); if (ft == 0) { MM(1 + q, (q ? g.wc : g.wb), g.x[1], 1 - q); } SB();
#pragma unroll
        for (int e = 0; e < 8; ++e) r[e] = __builtin_amdgcn_rcpf(__builtin_fmaf(ex[e], __builtin_fmaf(ex[e], 0.5f, 1.f), 1.f));
        SB(); LDW((li + 1) / 5, (li + 1) % 5); if (ft == 0) { MM(1 + q, (q ? g.wc : g.wb), g.x[1], 2 - q); } SB();
#pragma unroll
        for (int e = 0; e < 4; ++e) o[4 * q + e] = pk(__builtin_fmaf(-z[2 * e], r[2 * e], z[2 * e]), __builtin_fmaf(-z[2 * e + 1], r[2 * e + 1], z[2 * e + 1]));
        SB(); LDW((li + 2) / 5, (li + 2) % 5); if (ft == 0) { MM(1 + q, (q ? g.wc : g.wb), g.x[1], 3 - q); LDW(2, q); } SB();
      }
      if (ft == 0) { MM(2, g.wc, g.x[1], 3); WAITL0(); BAR(); SB(); }
      const u32 so = WS_ACT + (m * 48 + j * 6 + 2 * ft + wn) * 4096;
      st16(v4u{o[0], o[1], o[2], o[3]}, rs, aoff, so);
      st16(v4u{o[4], o[5], o[6], o[7]}, rs, aoff + 256, so);
    }
  }
#define GLW(i) do { if ((i) % 4 == 0) setm0(ldsb + w * 15360 + (i) * 1024); \
    asm volatile("buffer_load_dwordx4 %0, %1, %2 offen offset:%3 sc1 lds" ::"v"(vo), "s"(rsS), \
                 "s"((kprod(6 + (i) / 5) ? wb1 : wb0) + kstep(6 + (i) / 5) * 5120 + ((i) % 5 - (i) % 4) * 1024), "i"((i) % 4 * 1024) : "memory"); } while (0)
  GLW(0); GLW(1);
  waitv<6 + 2 + 2>();
  put_flag2(rs, vo ? WS_END : 0, WS_FG + ((m * 8 + j) * 4 + w) * 128, tag);
#pragma unroll
  for (int i = 2; i < 8; ++i) GLW(i);
  waitv<8 + 1>();
  put_flag2(rs, vo ? WS_END : 0, WS_FG2 + ((m * 8 + j) * 4 + w) * 128, tag);
#pragma unroll
  for (int i = 8; i < 15; ++i) GLW(i);
  {
    v4u f;
    asm volatile("buffer_load_dwordx4 %0, %1, %2, %3 offen sc1" : "=&v"(f) : "v"(FGO), "s"(rsS), "s"(WS_FG + (m * 8 + 2 * w) * 512) : "memory");
    const int lane = relane(vo);
#pragma unroll
    for (int i = 0; i < 3; ++i) {
      const u32 z = i * 64 + lane, r = (z * 6554) >> 16;
      setm0(ldsb + G_XT + w * 2560 + i * 1024);
      if (i < 2 || lane < 32) glds<0>(r * 1120 + z * 16, g.rsX, (64 * m + 16 * w) * 1280 + j * 160);
    }
    asm volatile("s_waitcnt vmcnt(3)" : "+v"(f)::"memory");
    if (!all_(match2(f, tag))) wait_flags<1, 4>(rs, FGO, WS_FG + (m * 8 + 2 * w) * 512, tag);
    order();
  }
  put_flag2(rs, (j | vo) ? WS_END : 0, WS_GD + (m * 4 + w) * 128, tag);
  __builtin_amdgcn_s_setprio(0);
  const u32 ab0 = WS_ACT + (m * 48 + 12 * w + 6 * par) * 4096, ab1 = WS_ACT + (m * 48 + 12 * w + 6 - 6 * par) * 4096;
  u32 to[4];
#pragma unroll
  for (int t = 0; t < 4; ++t) to[t] = ((w + 1 + t) & 3) * 1024;
  v4u af[12][4];
#define LDA(k, t) af[k][t] = ld16(rs, vo, (kprod(k) ? ab1 : ab0) + kstep(k) * 4096 + to[t])
#pragma unroll
  for (int t = 0; t < 8; ++t) LDA(t >> 2, t & 3);
  SB();
  f4 acc[4][5];
  v4u sv[3], f2v;
  LAS unsigned char* wl = lds + w * 15360 + vo;
#pragma unroll
  for (int k = 0; k < 9; ++k) {
#pragma unroll
    for (int n = 0; n < 5; ++n) {
      if (k == 0) mm4z(acc[0][n], acc[1][n], acc[2][n], acc[3][n], wf[k][n], af[k]);
      else mm4(acc[0][n], acc[1][n], acc[2][n], acc[3][n], wf[k][n], af[k]);
      SB();
      if (n < 4) {
        if (k < 2) LDA(k + 2, n);
        if (k >= 1 && k < 6) LDA(k + 3, n);
        if (k >= 6) LDA(9 + ((k - 6) * 4 + n) % 3, ((k - 6) * 4 + n) / 3);
      }
      if (k == 3 || k == 4) LDW(k + 6, n);
      if (k == 7) LDW(11, n);
      if (k >= 5 && k < 8) wf[k + 1][n] = *(LAS v4u*)(wl + ((k - 5) * 5 + n) * 1024);
      SB();
    }
    if (k == 2) f2v = ld16(rs, FGO, WS_FG2 + (m * 8 + 2 * w) * 512);
    if (k == 4) {
      if (!all_(match2(f2v, tag))) wait_flags<1, 4>(rs, FGO, WS_FG2 + (m * 8 + 2 * w) * 512, tag);
      order();
    }
    if (k == 6) f2v = ld16(rs, FSO, WS_FS + (j * 80 + w * 8) * 128);
    SB();
  }
#define LDS_(lane) _Pragma("unroll") for (int i = 0; i < 3; ++i) \
    sv[i] = ld16(rs, (i * 32 + w * 8 + ((lane) >> 3)) * 4096 + ((lane) & 7) * 16, (i < 2 || w < 2) ? WS_ST + j * 80 * 4096 + m * 128 : WS_END)
  const bool early = all_(match2(f2v, tag));
  order();
  if (early) { const int lane = relane(vo); LDS_(lane); }
  else f2v = ld16(rs, FSO, WS_FS + (j * 80 + w * 8) * 128);
  SB();
  {
    const int lane = relane(vo), tid = w * 64 + lane, g4 = lane >> 4;
    const u32 ob = ldsb + w * 15360 + vo;
    LAS unsigned char* P = lds + G_XT + w * 2560;
    u32 po = (lane & 15) * 160 + g4 * 8;
    const int row = 4 * g4 + ((lane & 15) >> 2);
    u32 spo = G_S + row * 128 + (((2 * w + ((lane & 3) >> 1)) ^ row) & 7) * 16 + (lane & 1) * 8;
    u32 oo[3];
    float xf[20];
    mm5(acc[0], wf[9], af[9][0]); SB();
    pinv(po); pinv(spo);
    v2u x2[5];
#pragma unroll
    for (int n = 0; n < 5; ++n) x2[n] = *(LAS v2u*)(P + po + n * 32);
    SB();
    mm5(acc[0], wf[10], af[10][0]); SB();
#pragma unroll
    for (int i = 0; i < 3; ++i) {
      const u32 z = i * 64 + lane, r = (z * 6554) >> 16;
      oo[i] = (i < 2 || lane < 32) ? r * 1120 + z * 16 : L * D * 2;
      pinv(oo[i]);
    }
    SB();
    mm5(acc[0], wf[11], af[11][0]); SB();
    const bool closer = b == 255 && w == 0;
    const v4u gd0 = ld16(rs, vo * 8, closer ? WS_GD : WS_END), gd1 = ld16(rs, vo * 8, closer ? WS_GD + 8192 : WS_END);
    u32 swo = ldsb + G_S + (tid >> 3) * 128 + (((tid & 7) ^ (tid >> 3)) & 7) * 16, pin[3];
    pinv(swo);
#pragma unroll
    for (int i = 0; i < 3; ++i) { pin[i] = ldsb + to[2 - i] * 15 + i * 5120 + vo; pinv(pin[i]); }
    SB();
#pragma unroll
    for (int t = 1; t < 4; ++t) {
      mm5(acc[t], wf[9], af[9][t]); SB();
      if (t == 1) {
#pragma unroll
        for (int n = 0; n < 5; ++n) { xf[4 * n] = bflo(x2[n].x); xf[4 * n + 1] = bfhi(x2[n].x); xf[4 * n + 2] = bflo(x2[n].y); xf[4 * n + 3] = bfhi(x2[n].y); }
#pragma unroll
        for (int i = 0; i < 20; ++i) pinv(xf[i]);
      }
      SB();
      mm5w(acc[t], wf[10], af[10][t], acc[t - 1], ob + (t - 1) * 5120); SB();
      mm5(acc[t], wf[11], af[11][t]); SB();
    }
    if (!early) {
      if (!all_(match2(f2v, tag))) wait_flags<1, 4>(rs, FSO, WS_FS + (j * 80 + w * 8) * 128, tag);
      order();
      LDS_(lane);
    }
    *(LAS v4u*)(size_t)swo = sv[0]; *(LAS v4u*)(size_t)(swo + 4096) = sv[1];
    if (w < 2) *(LAS v4u*)(size_t)(swo + 8192) = sv[2];
    const bool closed = closer && all_(match2(gd0, tag) && match2(gd1, tag));
    put_flag(rs, closed && !vo ? 0 : WS_END, 0, tag);
    syncl();
    f4 part[15]; v2u s2[5];
#pragma unroll
    for (int n = 0; n < 5; ++n) {
#pragma unroll
      for (int i = 0; i < 3; ++i) part[3 * n + i] = *(LAS f4*)(size_t)(pin[i] + n * 1024);
      SB();
    }
#pragma unroll
    for (int n = 0; n < 5; ++n) s2[n] = trd(lds + spo + n * 2048);
    SB();
    f4 z[5], e[5];
#pragma unroll
    for (int n = 0; n < 5; ++n) {
      z[n] = (acc[3][n] + part[3 * n]) + (part[3 * n + 1] + part[3 * n + 2]);
      SB();
      if (n) {
#pragma unroll
        for (int q = 0; q < 4; ++q) e[n - 1][q] += 1.f;
      }
#pragma unroll
      for (int q = 0; q < 4; ++q) e[n][q] = __builtin_amdgcn_exp2f(-z[n][q]);
      SB();
    }
#pragma unroll
    for (int n = 0; n < 5; ++n) {
#pragma unroll
      for (int q = 0; q < 4; ++q) e[n][q] = __builtin_amdgcn_rcpf(n == 4 ? e[n][q] + 1.f : e[n][q]);
#pragma unroll
      for (int q = 0; q < 4; ++q) z[n][q] *= (q & 1) ? bfhi(s2[n][q >> 1]) : bflo(s2[n][q >> 1]);
      SB();
      *(LAS v2u*)(P + po + n * 32) = v2u{pk(__builtin_fmaf(z[n][0], e[n][0], xf[4 * n]), __builtin_fmaf(z[n][1], e[n][1], xf[4 * n + 1])),
                                         pk(__builtin_fmaf(z[n][2], e[n][2], xf[4 * n + 2]), __builtin_fmaf(z[n][3], e[n][3], xf[4 * n + 3]))};
      SB();
    }
    const rsrc_t ro = mkr(p.out, L * D * 2);
    v4u q[3];
#pragma unroll
    for (int i = 0; i < 3; ++i) q[i] = *(LAS v4u*)(P + i * 1024 + vo);
#pragma unroll
    for (int i = 0; i < 3; ++i) st16<0>(q[i], ro, oo[i], (64 * m + 16 * w) * 1280 + j * 160);
    if (closer && !closed) {
      wait_flags<2, 4, 8192>(rs, vo * 8, WS_GD, tag);
      put_flag(rs, vo ? WS_END : 0, 0, tag);
    }
  }
}

constexpr u32 C_LN = 0, C_TAG = 8 * 1344, C_W2 = C_TAG + 64, C_W1 = C_W2 + 8192, C_TAB = C_W1 + 8192, C_TABSZ = 6784, C_U = C_TAB + 3 * C_TABSZ,
              C_USZ = 4608, C_END = C_U + 3 * C_USZ;
static_assert(C_END <= LDS_BYTES, "lds");
constexpr u32 T_RA = 64, T_RB = T_RA + 2240, T_CMP = T_RB + 2176, T_P3 = T_CMP + 1088;

DI float halfsum(float v) {
  v += dpp<0xB1>(v); v += dpp<0x4E>(v); v += dpp<0x141>(v); v += dpp<0x140>(v);
  const f2 t = swap16(v);
  return t.x + t.y;
}
DI u32 ln_part(const v2u (&a)[5], LAS unsigned char* row) {
  float v[20], s0 = 0.f, s1 = 0.f;
#pragma unroll
  for (int i = 0; i < 10; ++i) { v[2 * i] = bflo(a[i >> 1][i & 1]); v[2 * i + 1] = bfhi(a[i >> 1][i & 1]); s0 += v[2 * i]; s1 += v[2 * i + 1]; }
  const float mean = halfsum(s0 + s1) * (1.f / D);
  s0 = s1 = 0.f;
#pragma unroll
  for (int i = 0; i < 10; ++i) {
    const float t0 = v[2 * i] - mean, t1 = v[2 * i + 1] - mean;
    s0 = __builtin_fmaf(t0, t0, s0); s1 = __builtin_fmaf(t1, t1, s1);
  }
  const float mb = rbf(mean);
  u32 c[10];
#pragma unroll
  for (int i = 0; i < 10; ++i) c[i] = pk(v[2 * i] - mb, v[2 * i + 1] - mb);
  const u32 st = pk(mean, __builtin_amdgcn_sqrtf(rbf(rbf(halfsum(s0 + s1) * (1.f / D)) + 1e-5f)));
  const float r = __builtin_amdgcn_rcpf(bfhi(st));
#pragma unroll
  for (int i = 0; i < 5; ++i)
    *(LAS v2u*)(row + i * 256) = v2u{pk(bflo(c[2 * i]) * r, bfhi(c[2 * i]) * r), pk(bflo(c[2 * i + 1]) * r, bfhi(c[2 * i + 1]) * r)};
  return st;
}

DI void publish(rsrc_t rs, LAS unsigned char* lds, int c, int tb, const u32 (&sw)[8], u32 sc, v2u tag, int lane) {
  const int g = lane >> 4, q4 = (lane & 15) >> 2;
  LAS unsigned char* qx = lds + C_LN + q4 * 1344 + g * 32 + 8 * (lane & 3);
  LAS unsigned char* qw = lds + C_W2 + (16 * (g & 1) + 4 * (g >> 1) + q4) * 256 + 8 * (lane & 3);
  LAS unsigned char* qc = lds + C_W1 + (4 * g + q4) * 128 + 8 * (lane & 3);
  v2u lo[10], hi[10], cl[8], ch[8];
  waitv<0>();
#pragma unroll
  for (int t = 0; t < 8; ++t) { lo[t] = trd(qw + t * 32); hi[t] = trd(qw + t * 32 + 8 * 256); }
#pragma unroll
  for (int t = 0; t < 8; ++t) { cl[t] = trd(qc + (t >> 1) * 32 + (t & 1) * 4096); ch[t] = trd(qc + (t >> 1) * 32 + (t & 1) * 4096 + 2048); }
  const u32 st = *(LAS u32*)(lds + C_TAG + 16 + (lane & 7) * 4);
  const v4u ones = {0x3f803f80u, 0x3f803f80u, 0x3f803f80u, 0x3f803f80u};
  f4 cs[4] = {};
#pragma unroll
  for (int t = 0; t < 8; ++t) {
    st16(v4u{lo[t].x, lo[t].y, hi[t].x, hi[t].y}, rs, lane * 16, sw[t]);
    if (t >= 4) cs[t - 4] = mfma16(v4u{cl[2 * t - 7].x, cl[2 * t - 7].y, ch[2 * t - 7].x, ch[2 * t - 7].y}, ones,
                                   mfma16(v4u{cl[2 * t - 8].x, cl[2 * t - 8].y, ch[2 * t - 8].x, ch[2 * t - 8].y}, ones, cs[t - 4]));
  }
#pragma unroll
  for (int i = 0; i < 10; ++i) { lo[i] = trd(qx + i * 128); hi[i] = trd(qx + i * 128 + 4 * 1344); }
  __builtin_amdgcn_raw_buffer_store_b32(st, rs, lane < 8 ? lane * 4 : WS_END, WS_STAT + tb * 32, SC1);
  const int t = lane & 3;
  const f4 o = t == 0 ? cs[0] : t == 1 ? cs[1] : t == 2 ? cs[2] : cs[3];
  st16(__builtin_bit_cast(v4u, o), rs, (lane & 12) ? WS_END : t * 64 + g * 16, sc);
#pragma unroll
  for (int i = 0; i < 10; ++i) {
    st16(v4u{lo[i].x, lo[i].y, hi[i].x, hi[i].y}, rs, lane * 16, WS_XNT + tb * 10240 + i * 1024);
    if (i == 5) {
      waitv<6>();
      put_flag2(rs, lane ? WS_END : 0, WS_FW + tb * 128, tag);
    }
  }
  waitv<0>();
  put_flag(rs, lane < 8 ? lane * 2048 : WS_END, WS_FA + c * 8, tag);
}

DI void gather_u(rsrc_t rs, LAS unsigned char* lds, int d0, bool two, int lane) {
  v4u v[12];
  u32 go[3], lo[3];
#pragma unroll
  for (int i = 0; i < 3; ++i) {
    const u32 q = i * 64 + lane, tb = (q * 21846) >> 16, ch = q - 3 * tb;
    go[i] = tb * 10240 + (two && ch == 2 ? 16 : ch * 16); lo[i] = C_U + ch * C_USZ + 480 + tb * 16;
  }
#pragma unroll
  for (int i = 0; i < 12; ++i) v[i] = ld16(rs, go[i % 3], WS_XNT + d0 * 16 + (i / 3) * 64 * 10240);
#pragma unroll
  for (int i = 0; i < 12; ++i) *(LAS v4u*)(lds + lo[i % 3] + (i / 3) * 1024) = v[i];
}

DI f2 cexp(float a2, float br) {
  const float e = __builtin_amdgcn_exp2f(a2), ph = __builtin_amdgcn_fractf(br);
  return f2{e * __builtin_amdgcn_cosf(ph), e * __builtin_amdgcn_sinf(ph)};
}
DI f2 csq(f2 x) { return f2{__builtin_fmaf(x.x, x.x, -(x.y * x.y)), (x.x + x.x) * x.y}; }
DI h2 toh(f2 x) { return __builtin_convertvector(x, h2); }
DI u32 hb(h2 x) { return __builtin_bit_cast(u32, x); }
struct HP { h2 re, im; };
DI HP pm(HP x, h2 m) { return HP{__builtin_elementwise_fma(x.im, -m.yy, x.re * m.xx), __builtin_elementwise_fma(x.im, m.xx, x.re * m.yy)}; }
DI HP pmc(HP x, h2 m) { return HP{__builtin_elementwise_fma(x.im, m.yy, x.re * m.xx), __builtin_elementwise_fma(x.re, -m.yy, x.im * m.xx)}; }
DI f4 mfma16h(v4u a, v4u b, f4 c) { return __builtin_amdgcn_mfma_f32_16x16x32_f16(__builtin_bit_cast(h8, a), __builtin_bit_cast(h8, b), c, 0, 0, 0); }
#define RE4(P, i) v4u{hb(P[i].re), hb(P[i + 1].re), hb(P[i + 2].re), hb(P[i + 3].re)}
#define IM4(P, i) v4u{hb(P[i].im), hb(P[i + 1].im), hb(P[i + 2].im), hb(P[i + 3].im)}
DI void tofrag(const f16v& d, v4u (&f)[2]) {
#pragma unroll
  for (int h = 0; h < 2; ++h)
#pragma unroll
    for (int e = 0; e < 4; ++e)
      f[h][e] = __builtin_bit_cast(u32, __builtin_amdgcn_cvt_pkrtz(d[8 * h + 2 * e], d[8 * h + 2 * e + 1]));
}

struct KPrep { v4u selL, selH; };
DI void kprep(KPrep& k, int lane) {
  const u32 pos = ((lane >> 4) & 1) == (lane >> 5) ? (lane & 15) >> 1 : 8, one = (lane & 1) ? 0x3c000000u : 0x00003c00u;
#pragma unroll
  for (int e = 0; e < 4; ++e) { k.selL[e] = pos == e ? one : 0u; k.selH[e] = pos == 4 + e ? one : 0u; }
  pinv(k.selL); pinv(k.selH);
}
DI void kgen(KPrep& kp, const u16 (&pr)[3][6], float dt, float dsk, LAS unsigned char* T, int lane) {
  float a2[3], br[3]; f2 W[3];
  float mx = 0.f;
#pragma unroll
  for (int c = 0; c < 3; ++c) {
    a2[c] = dt * bf2f(pr[c][0]) * 1.44269504f; br[c] = dt * bf2f(pr[c][1]) * 0.15915494f;
    const float Br = bf2f(pr[c][2]), Bi = bf2f(pr[c][3]), Cr = bf2f(pr[c][4]), Ci = bf2f(pr[c][5]);
    W[c] = f2{Cr * Br - Ci * Bi, -(Cr * Bi + Ci * Br)};
    mx = __builtin_fmaxf(mx, __builtin_fmaxf(__builtin_fabsf(W[c].x), __builtin_fabsf(W[c].y)));
  }
  const int ex = __builtin_amdgcn_frexp_expf(wavemax(mx)) - 10;
#pragma unroll
  for (int c = 0; c < 3; ++c) W[c] = f2{__builtin_amdgcn_ldexpf(W[c].x, -ex), __builtin_amdgcn_ldexpf(W[c].y, -ex)};
  const v4u selL = kp.selL, selH = kp.selH;
  f4 acc[2][2] = {};
  const f16v Z = {};
  SB();
  __builtin_amdgcn_s_setprio(1);
#define KM(ta, tb, c, h) acc[ta][tb] = mfma16h(fa[ta][c][h], fb[c][h], acc[ta][tb])
#pragma unroll 1
  for (int ck = 0; ck < 3; ++ck) {
    v4u fa[2][2][2], fb[2][2];
    const f2 E1 = cexp(a2[0], br[0]), H1 = cexp(a2[0] * 256.f, br[0] * 256.f);
    const f2 E2 = csq(E1), H2 = csq(H1), E4 = csq(E2), H4 = csq(H2);
    HP U[12];
    const h2 e1 = toh(E1), e2 = toh(E2), h1 = toh(H1), h2_ = toh(H2), h4 = toh(H4);
    U[0] = HP{toh(f2{W[0].x, W[0].x * E4.x + W[0].y * E4.y}), toh(f2{W[0].y, W[0].y * E4.x - W[0].x * E4.y})};
    U[1] = pmc(U[0], e1); U[2] = pmc(U[0], e2); U[3] = pmc(U[2], e1);
    SB();
    f16v dr = mfma32h(RE4(U, 0), selL, Z); SB();
    U[4] = pmc(U[0], h1); U[5] = pmc(U[2], h1); SB();
    f16v di = mfma32h(IM4(U, 0), selL, Z); SB();
    U[6] = pmc(U[0], h2_); U[7] = pmc(U[4], h2_); SB();
    dr = mfma32h(RE4(U, 4), selH, dr); SB();
    U[8] = pmc(HP{h2{U[0].re.x, U[4].re.x}, h2{U[0].im.x, U[4].im.x}}, h4); SB();
    di = mfma32h(IM4(U, 4), selH, di); SB();
    U[9] = pmc(U[8], h2_); U[10] = pmc(U[8], h4); U[11] = HP{h2{0, 0}, h2{0, 0}}; SB();
    f16v er = mfma32h(RE4(U, 8), selL, Z); SB();
    const f2 E8 = cexp(a2[0] * 8.f, br[0] * 8.f); SB();
    f16v ei = mfma32h(IM4(U, 8), selL, Z); SB();
    const f2 E16 = csq(E8), E32 = csq(E16), E64 = csq(E32), E128 = csq(E64);
    HP P[16];
    P[0] = HP{toh(f2{1.f, E8.x}), toh(f2{0.f, E8.y})};
    const h2 e16 = toh(E16), e32 = toh(E32), e64 = toh(E64), e128 = toh(E128);
    tofrag(dr, fa[0][0]);
    P[1] = pm(P[0], e16); P[2] = pm(P[0], e32); P[3] = pm(P[1], e32);
    SB();
    dr = mfma32h(RE4(P, 0), selL, Z); SB();
    tofrag(di, fa[0][1]); SB();
    di = mfma32h(IM4(P, 0), selL, Z); SB();
    P[4] = pm(P[0], e64); P[5] = pm(P[1], e64); P[6] = pm(P[2], e64); P[7] = pm(P[3], e64); SB();
    dr = mfma32h(RE4(P, 4), selH, dr); SB();
    tofrag(er, fa[1][0]); SB();
    di = mfma32h(IM4(P, 4), selH, di); SB();
    tofrag(ei, fa[1][1]);
    P[8] = pm(P[0], e128); P[9] = pm(P[1], e128); P[10] = pm(P[2], e128); P[11] = pm(P[3], e128); SB();
    er = mfma32h(RE4(P, 8), selL, Z); SB();
    P[12] = pm(P[4], e128); P[13] = pm(P[5], e128); SB();
    ei = mfma32h(IM4(P, 8), selL, Z); SB();
    P[14] = pm(P[6], e128); P[15] = pm(P[7], e128); SB();
    er = mfma32h(RE4(P, 12), selH, er); SB();
    tofrag(dr, fb[0]); SB();
    ei = mfma32h(IM4(P, 12), selH, ei); SB();
    tofrag(di, fb[1]); SB();
    KM(0, 0, 0, 0); KM(1, 0, 0, 0); KM(0, 0, 0, 1); KM(1, 0, 0, 1);
    tofrag(er, fb[0]);
    KM(0, 0, 1, 0); KM(1, 0, 1, 0); KM(0, 0, 1, 1); KM(1, 0, 1, 1);
    __builtin_amdgcn_sched_group_barrier(8, 4, 0);
#pragma unroll
    for (int q = 0; q < 4; ++q) { __builtin_amdgcn_sched_group_barrier(8, 1, 0); __builtin_amdgcn_sched_group_barrier(2, 2, 0); }
    SB();
    tofrag(ei, fb[1]);
    KM(0, 1, 0, 0); KM(1, 1, 0, 0); KM(0, 1, 0, 1); KM(1, 1, 0, 1);
#pragma unroll
    for (int q = 0; q < 4; ++q) { __builtin_amdgcn_sched_group_barrier(2, 2, 1); __builtin_amdgcn_sched_group_barrier(8, 1, 1); }
    SB();
    KM(0, 1, 1, 0); KM(1, 1, 1, 0); KM(0, 1, 1, 1); KM(1, 1, 1, 1);
    SB();
    a2[0] = a2[1]; a2[1] = a2[2]; br[0] = br[1]; br[1] = br[2]; W[0] = W[1]; W[1] = W[2];
    SB();
  }
  const int gq = lane >> 4;
  const float sc = __builtin_amdgcn_ldexpf(dt, ex) * 0.69314718f;
  LAS unsigned char* C = T + T_CMP;
  {
    const u32 sh = gq < 2 ? 4 : 5 - gq;
    const u32 o0 = ((lane & 15) << sh) + (gq < 2 ? 4 * gq : 256 * gq), o1 = o0 + (gq == 3 ? 128 : 8 >> (gq >> 1)), st = 16 << sh;
#pragma unroll
    for (int t = 0; t < 2; ++t) {
      float k0 = acc[0][t][0] * sc;
      const float k1 = acc[0][t][1] * sc, k2 = acc[0][t][2] * sc, k3 = acc[0][t][3] * sc;
      if (t == 0 && lane == 0) k0 += dsk;
      *(LAS u32*)(C + o0 + t * st) = pk(k0, gq == 3 ? k1 : k2);
      *(LAS u32*)(C + o1 + t * st) = pk(gq == 3 ? k2 : k1, k3);
#pragma unroll
      for (int r = 0; r < 4; ++r) *(LAS u16*)(T + T_P3 + 58 + (128 * gq + (lane & 15)) * 2 + (32 * r + 16 * t) * 2) = (u16)pk(acc[1][t][r] * sc, 0.f);
    }
  }
  {
    LAS unsigned char* q2 = C + (63 - lane) * 4;
    const u32 k1024 = *(LAS u16*)(T + T_P3 + 58);
    const u32 cb4 = *(LAS u32*)(q2 + 768), cb2 = *(LAS u32*)(q2 + 512), a2_ = *(LAS u16*)(q2 + 516);
    u32 a4 = *(LAS u16*)(q2 + 772);
    const u32 c2x = *(LAS u16*)(q2 + (lane == 63 ? 510 : 512));
    const v2u k03 = *(LAS v2u*)(C + (63 - lane) * 8);
    u32 km = *(LAS u16*)(C + (63 - lane) * 8 - 2);
    if (lane == 63) km = 0;
    if (lane == 0) a4 = k1024;
    LAS unsigned char* o = T + lane * 16;
    const u32 bb = __builtin_amdgcn_perm(cb4, cb4, 0x03020302u), cc = __builtin_amdgcn_perm(cb4, cb4, 0x01000100u);
    *(LAS v4u*)(o + T_RA + 64) = v4u{a4 * 0x10001u, bb, __builtin_amdgcn_alignbit(cb4, cb4, 16), cc};
    *(LAS v4u*)(o + T_RB + 64) = v4u{a4 | (cb4 & 0xffff0000u), bb, cc, cc};
    o = T + lane * 8;
    *(LAS v2u*)(o + T_RA + 1088) = v2u{a2_ | (cb2 & 0xffff0000u), __builtin_amdgcn_perm(cb2, cb2, 0x01000100u)};
    *(LAS v2u*)(o + T_RB + 1088) = v2u{__builtin_amdgcn_alignbit(cb2, cb2, 16), (cb2 & 0xffffu) | (c2x << 16)};
    *(LAS v2u*)(o + T_RA + 1600) = v2u{__builtin_amdgcn_alignbit(k03.y, k03.y, 16), __builtin_amdgcn_alignbit(k03.x, k03.x, 16)};
    *(LAS v2u*)(o + T_RB + 1600) = v2u{(k03.y & 0xffffu) | (k03.x & 0xffff0000u), (k03.x & 0xffffu) | (km << 16)};
    const u32 e = lane & 15;
    const u32 ea = gq == 0 ? T_RA + 4 * e : gq == 1 ? T_RB + 4 * e : gq == 2 ? (e < 8 ? T_RA : T_RB - 32) + 2112 + 4 * e : T_P3 - 4 + 4 * (15 - e);
    const u32 ev = gq >= 2 || e < 13 ? 0 : (lane == 13 ? k1024 << 16 : k1024 * 0x10001u);
    *(LAS u32*)(T + ea) = ev;
  }
}

#define CONV_ASM \
  "ds_read2_b32 v[160:161], %10 offset0:160 offset1:161\n\t" \
  "ds_read2_b32 v[162:163], %10 offset0:162 offset1:163\n\t" \
  "ds_read_b128 v[176:179], %11 offset:128\n\t" \
  "ds_read_b128 v[180:183], %11 offset:640\n\t" \
  "ds_read_b128 v[184:187], %11 offset:1152\n\t" \
  "ds_read_b128 v[188:191], %11 offset:1664\n\t" \
  "ds_read_b128 v[192:195], %11 offset:2176\n\t" \
  "ds_read_b128 v[196:199], %11 offset:2688\n\t" \
  "ds_read_b128 v[200:203], %11 offset:3200\n\t" \
  "ds_read_b128 v[204:207], %11 offset:3712\n\t" \
  "ds_read2_b32 v[164:165], %10 offset0:32 offset1:33\n\t" \
  "ds_read2_b32 v[166:167], %10 offset0:34 offset1:35\n\t" \
  "ds_read2_b32 v[168:169], %9 offset0:160 offset1:161\n\t" \
  "ds_read2_b32 v[170:171], %9 offset0:162 offset1:163\n\t" \
  "ds_read2_b32 v[172:173], %9 offset0:32 offset1:33\n\t" \
  "ds_read2_b32 v[174:175], %9 offset0:34 offset1:35\n\t" \
  "1:\n\t" \
  "s_waitcnt lgkmcnt(0)\n\t" \
  "v_mfma_f32_16x16x32_bf16 %0, v[160:163], v[176:179], %0\n\t" \
  "ds_read2_b32 v[208:209], %10 offset0:144 offset1:145\n\t" \
  "v_mfma_f32_16x16x32_bf16 %1, v[160:163], v[180:183], %1\n\t" \
  "ds_read2_b32 v[210:211], %10 offset0:146 offset1:147\n\t" \
  "v_mfma_f32_16x16x32_bf16 %2, v[160:163], v[184:187], %2\n\t" \
  "ds_read_b128 v[224:227], %11 offset:64\n\t" \
  "v_mfma_f32_16x16x32_bf16 %3, v[160:163], v[188:191], %3\n\t" \
  "ds_read_b128 v[228:231], %11 offset:576\n\t" \
  "v_mfma_f32_16x16x32_bf16 %4, v[160:163], v[192:195], %4\n\t" \
  "ds_read_b128 v[232:235], %11 offset:1088\n\t" \
  "v_mfma_f32_16x16x32_bf16 %5, v[160:163], v[196:199], %5\n\t" \
  "ds_read_b128 v[236:239], %11 offset:1600\n\t" \
  "v_mfma_f32_16x16x32_bf16 %6, v[160:163], v[200:203], %6\n\t" \
  "ds_read_b128 v[240:243], %11 offset:2112\n\t" \
  "v_mfma_f32_16x16x32_bf16 %7, v[160:163], v[204:207], %7\n\t" \
  "ds_read_b128 v[244:247], %11 offset:2624\n\t" \
  "v_mfma_f32_16x16x32_bf16 %1, v[164:167], v[176:179], %1\n\t" \
  "ds_read_b128 v[248:251], %11 offset:3136\n\t" \
  "v_mfma_f32_16x16x32_bf16 %2, v[164:167], v[180:183], %2\n\t" \
  "ds_read_b128 v[252:255], %11 offset:3648\n\t" \
  "v_mfma_f32_16x16x32_bf16 %3, v[164:167], v[184:187], %3\n\t" \
  "ds_read2_b32 v[212:213], %10 offset0:16 offset1:17\n\t" \
  "v_mfma_f32_16x16x32_bf16 %4, v[164:167], v[188:191], %4\n\t" \
  "ds_read2_b32 v[214:215], %10 offset0:18 offset1:19\n\t" \
  "v_mfma_f32_16x16x32_bf16 %5, v[164:167], v[192:195], %5\n\t" \
  "ds_read2_b32 v[216:217], %9 offset0:144 offset1:145\n\t" \
  "v_mfma_f32_16x16x32_bf16 %6, v[164:167], v[196:199], %6\n\t" \
  "ds_read2_b32 v[218:219], %9 offset0:146 offset1:147\n\t" \
  "v_mfma_f32_16x16x32_bf16 %7, v[164:167], v[200:203], %7\n\t" \
  "ds_read2_b32 v[220:221], %9 offset0:16 offset1:17\n\t" \
  "v_mfma_f32_16x16x32_bf16 %2, v[168:171], v[176:179], %2\n\t" \
  "ds_read2_b32 v[222:223], %9 offset0:18 offset1:19\n\t" \
  "v_mfma_f32_16x16x32_bf16 %3, v[168:171], v[180:183], %3\n\t" \
  "v_mfma_f32_16x16x32_bf16 %4, v[168:171], v[184:187], %4\n\t" \
  "v_mfma_f32_16x16x32_bf16 %5, v[168:171], v[188:191], %5\n\t" \
  "v_mfma_f32_16x16x32_bf16 %6, v[168:171], v[192:195], %6\n\t" \
  "v_mfma_f32_16x16x32_bf16 %7, v[168:171], v[196:199], %7\n\t" \
  "v_mfma_f32_16x16x32_bf16 %3, v[172:175], v[176:179], %3\n\t" \
  "v_mfma_f32_16x16x32_bf16 %4, v[172:175], v[180:183], %4\n\t" \
  "v_mfma_f32_16x16x32_bf16 %5, v[172:175], v[184:187], %5\n\t" \
  "v_mfma_f32_16x16x32_bf16 %6, v[172:175], v[188:191], %6\n\t" \
  "v_mfma_f32_16x16x32_bf16 %7, v[172:175], v[192:195], %7\n\t" \
  "s_waitcnt lgkmcnt(0)\n\t" \
  "v_mfma_f32_16x16x32_bf16 %0, v[208:211], v[224:227], %0\n\t" \
  "ds_read2_b32 v[160:161], %10 offset0:128 offset1:129\n\t" \
  "v_mfma_f32_16x16x32_bf16 %1, v[208:211], v[228:231], %1\n\t" \
  "ds_read2_b32 v[162:163], %10 offset0:130 offset1:131\n\t" \
  "v_mfma_f32_16x16x32_bf16 %2, v[208:211], v[232:235], %2\n\t" \
  "ds_read_b128 v[176:179], %11 offset:0\n\t" \
  "v_mfma_f32_16x16x32_bf16 %3, v[208:211], v[236:239], %3\n\t" \
  "ds_read_b128 v[180:183], %11 offset:512\n\t" \
  "v_mfma_f32_16x16x32_bf16 %4, v[208:211], v[240:243], %4\n\t" \
  "ds_read_b128 v[184:187], %11 offset:1024\n\t" \
  "v_mfma_f32_16x16x32_bf16 %5, v[208:211], v[244:247], %5\n\t" \
  "ds_read_b128 v[188:191], %11 offset:1536\n\t" \
  "v_mfma_f32_16x16x32_bf16 %6, v[208:211], v[248:251], %6\n\t" \
  "ds_read_b128 v[192:195], %11 offset:2048\n\t" \
  "v_mfma_f32_16x16x32_bf16 %7, v[208:211], v[252:255], %7\n\t" \
  "ds_read_b128 v[196:199], %11 offset:2560\n\t" \
  "v_mfma_f32_16x16x32_bf16 %1, v[212:215], v[224:227], %1\n\t" \
  "ds_read_b128 v[200:203], %11 offset:3072\n\t" \
  "v_mfma_f32_16x16x32_bf16 %2, v[212:215], v[228:231], %2\n\t" \
  "ds_read_b128 v[204:207], %11 offset:3584\n\t" \
  "v_mfma_f32_16x16x32_bf16 %3, v[212:215], v[232:235], %3\n\t" \
  "ds_read2_b32 v[164:165], %10 offset0:0 offset1:1\n\t" \
  "v_mfma_f32_16x16x32_bf16 %4, v[212:215], v[236:239], %4\n\t" \
  "ds_read2_b32 v[166:167], %10 offset0:2 offset1:3\n\t" \
  "v_mfma_f32_16x16x32_bf16 %5, v[212:215], v[240:243], %5\n\t" \
  "ds_read2_b32 v[168:169], %9 offset0:128 offset1:129\n\t" \
  "v_mfma_f32_16x16x32_bf16 %6, v[212:215], v[244:247], %6\n\t" \
  "ds_read2_b32 v[170:171], %9 offset0:130 offset1:131\n\t" \
  "v_mfma_f32_16x16x32_bf16 %7, v[212:215], v[248:251], %7\n\t" \
  "ds_read2_b32 v[172:173], %9 offset0:0 offset1:1\n\t" \
  "v_mfma_f32_16x16x32_bf16 %2, v[216:219], v[224:227], %2\n\t" \
  "ds_read2_b32 v[174:175], %9 offset0:2 offset1:3\n\t" \
  "v_mfma_f32_16x16x32_bf16 %3, v[216:219], v[228:231], %3\n\t" \
  "v_subrev_u32 %9, 128, %9\n\t" \
  "v_mfma_f32_16x16x32_bf16 %4, v[216:219], v[232:235], %4\n\t" \
  "v_subrev_u32 %10, 128, %10\n\t" \
  "v_mfma_f32_16x16x32_bf16 %5, v[216:219], v[236:239], %5\n\t" \
  "v_subrev_u32 %11, 128, %11\n\t" \
  "v_mfma_f32_16x16x32_bf16 %6, v[216:219], v[240:243], %6\n\t" \
  "s_add_i32 %8, %8, -1\n\t" \
  "v_mfma_f32_16x16x32_bf16 %7, v[216:219], v[244:247], %7\n\t" \
  "s_cmp_lg_u32 %8, 0\n\t" \
  "v_mfma_f32_16x16x32_bf16 %3, v[220:223], v[224:227], %3\n\t" \
  "v_mfma_f32_16x16x32_bf16 %4, v[220:223], v[228:231], %4\n\t" \
  "v_mfma_f32_16x16x32_bf16 %5, v[220:223], v[232:235], %5\n\t" \
  "v_mfma_f32_16x16x32_bf16 %6, v[220:223], v[236:239], %6\n\t" \
  "v_mfma_f32_16x16x32_bf16 %7, v[220:223], v[240:243], %7\n\t" \
  "s_cbranch_scc1 1b\n\t" \
  "s_waitcnt lgkmcnt(0)\n\t" \
  "s_nop 7\n\t" \
  ""
#define CONV_CLOB "v160", "v161", "v162", "v163", "v164", "v165", "v166", "v167", "v168", "v169", "v170", "v171", "v172", "v173", "v174", "v175", "v176", "v177", "v178", "v179", "v180", "v181", "v182", "v183", "v184", "v185", "v186", "v187", "v188", "v189", "v190", "v191", "v192", "v193", "v194", "v195", "v196", "v197", "v198", "v199", "v200", "v201", "v202", "v203", "v204", "v205", "v206", "v207", "v208", "v209", "v210", "v211", "v212", "v213", "v214", "v215", "v216", "v217", "v218", "v219", "v220", "v221", "v222", "v223", "v224", "v225", "v226", "v227", "v228", "v229", "v230", "v231", "v232", "v233", "v234", "v235", "v236", "v237", "v238", "v239", "v240", "v241", "v242", "v243", "v244", "v245", "v246", "v247", "v248", "v249", "v250", "v251", "v252", "v253", "v254", "v255"

DI void conv(rsrc_t rs, LAS unsigned char* T, LAS unsigned char* U, int d, int lane) {
  const int r = lane & 15, g = lane >> 4;
  v4u b7, b9;
  {
    const u32 m9 = (u32)((0x1ffull << (r + 4)) >> (8 * g)), m7 = (u32)((0x7full << (r + 5)) >> (8 * g));
#pragma unroll
    for (int e = 0; e < 4; ++e) {
      b9[e] = (((m9 >> (2 * e)) & 1) | (((m9 >> (2 * e + 1)) & 1) << 16)) * 0x3f80u;
      b7[e] = (((m7 >> (2 * e)) & 1) | (((m7 >> (2 * e + 1)) & 1) << 16)) * 0x3f80u;
    }
  }
  LAS unsigned char* ap = T + ((r & 1) ? T_RA : T_RB) + (((2031 - r + 8 * g) >> 1) - 496) * 4;
  LAS unsigned char* bq = U + (224 + 16 * r + 8 * g) * 2;
  f4 acc[8];
  {
    v4u u7[4], u9[4], kf[4];
#pragma unroll
    for (int i = 0; i < 4; ++i) { u7[i] = *(LAS v4u*)(bq + i * 512); u9[i] = *(LAS v4u*)(bq + i * 512 - 16); }
    const LAS u32* a32 = (const LAS u32*)(ap - 2048);
    const v4u A32 = {a32[0], a32[1], a32[2], a32[3]};
    const LAS u32* kq = (const LAS u32*)(T + T_P3) + (12 + r - 4 * g);
#pragma unroll
    for (int i = 0; i < 4; ++i) kf[i] = v4u{kq[16 * i], kq[16 * i + 1], kq[16 * i + 2], kq[16 * i + 3]};
    v4u xf[4];
#pragma unroll
    for (int i = 0; i < 4; ++i) {
      const f4 x7 = mfma16(u7[i], b7, f4{}), x9 = mfma16(u9[i], b9, f4{});
      acc[i] = f4{};
      acc[4 + i] = mfma16(A32, u7[i], f4{});
      xf[i] = v4u{pk(x7[3], x9[3]), pk(x7[2], x9[2]), pk(x7[1], x9[1]), pk(x7[0], x9[0])};
    }
#pragma unroll
    for (int i = 0; i < 4; ++i)
#pragma unroll
      for (int q = 0; q + i < 4; ++q) acc[4 + i + q] = mfma16(xf[i], kf[q], acc[4 + i + q]);
  }
  u32 a0 = (u32)(size_t)ap - 3 * 512 - 128, a1 = a0 + 1024, bp = (u32)(size_t)bq - 128;
  int n = 4;
  asm volatile(CONV_ASM
               : "+v"(acc[0]), "+v"(acc[1]), "+v"(acc[2]), "+v"(acc[3]), "+v"(acc[4]), "+v"(acc[5]), "+v"(acc[6]), "+v"(acc[7]), "+s"(n),
                 "+v"(a0), "+v"(a1), "+v"(bp)
               :
               : "memory", "scc", CONV_CLOB);
#pragma unroll
  for (int c = 0; c < 8; c += 2) {
    const v2u lo = __builtin_amdgcn_permlane16_swap(pk(acc[c][0], acc[c][1]), pk(acc[c + 1][0], acc[c + 1][1]), false, false);
    const v2u hi = __builtin_amdgcn_permlane16_swap(pk(acc[c][2], acc[c][3]), pk(acc[c + 1][2], acc[c + 1][3]), false, false);
    st16(v4u{lo.x, hi.x, lo.y, hi.y}, rs, (g & 1) * 512 + r * 32 + (g >> 1) * 16 + c * 512, WS_ST + d * 4096);
  }
}

DI void c_role(const Params& p, LAS unsigned char* lds, int c, int tid) {
  const int lane = tid & 63, w = __builtin_amdgcn_readfirstlane(tid >> 6);
  const int xc = c & 7, r = c >> 3, tb = 32 * xc + r, r3 = (r * 86) >> 8, d0 = 80 * xc + 3 * r - r3, d = d0 + w;
  const bool chan = r < 30 && w < (r - 3 * r3 == 2 ? 2 : 3);
  __builtin_amdgcn_s_setprio(3);
  asm volatile("ds_write_b32 %0, %1\n\ts_waitcnt lgkmcnt(0)\n\ts_barrier" ::"v"((u32)(size_t)lds + C_TAG + 48), "v"(0u) : "memory");
  v2u xa[5], pq[6];
  {
    const v4u rx = mkrsrc(p.x, L * D * 2);
    asm volatile("buffer_load_dwordx2 %0, %5, %6, %7 offen\n\tbuffer_load_dwordx2 %1, %5, %6, %7 offen offset:256\n\t"
                 "buffer_load_dwordx2 %2, %5, %6, %7 offen offset:512\n\tbuffer_load_dwordx2 %3, %5, %6, %7 offen offset:768\n\t"
                 "buffer_load_dwordx2 %4, %5, %6, %7 offen offset:1024"
                 : "=&v"(xa[0]), "=&v"(xa[1]), "=&v"(xa[2]), "=&v"(xa[3]), "=&v"(xa[4])
                 : "v"((tid >> 5) * 1280 + (tid & 31) * 8), "s"(rx), "s"(tb * 10240) : "memory");
    asm volatile("global_load_dwordx2 %0, %6, %7 sc1\n\tglobal_load_dwordx2 %1, %6, %8\n\tglobal_load_dwordx2 %2, %6, %9\n\t"
                 "global_load_dwordx2 %3, %6, %10\n\tglobal_load_dwordx2 %4, %6, %11\n\tglobal_load_dwordx2 %5, %6, %12"
                 : "=&v"(pq[0]), "=&v"(pq[1]), "=&v"(pq[2]), "=&v"(pq[3]), "=&v"(pq[4]), "=&v"(pq[5])
                 : "v"(chan ? d * 384 + ((6 * lane) & ~3) : 0), "s"(w == 3 ? (const u16*)p.ws : p.Are), "s"(p.Aim), "s"(p.Bre), "s"(p.Bim), "s"(p.Cre), "s"(p.Cim) : "memory");
  }
  const int rp = r < 30 ? r : 0, r5 = (rp * 52) >> 8, ks = 6 * xc + r5, cg = rp - 5 * r5, kc = rp - 10 * (r5 >> 1), strip = 12 * (xc & 1) + 3 * (xc >> 1) + (r5 >> 1);
  if (w == 3) {
    const v4u r1 = mkrsrc(p.w1, F * D * 2), rw = mkrsrc(p.w2, F * D * 2);
    const u32 v1 = (lane >> 3) * 3072 + (lane & 7) * 16, v2 = (lane >> 4) * 1280 + (lane & 15) * 16;
    const u32 s1 = kc * 64 * 3072 + strip * 128, s2 = ks * 32 * 1280 + cg * 256;
#pragma unroll
    for (int i = 0; i < 2; ++i) {
      setm0((u32)(size_t)lds + C_W1 + i * 4096);
      glds<0>(v1, r1, s1 + i * 32 * 3072); glds<1024>(v1, r1, s1 + (i * 32 + 8) * 3072 - 1024);
      glds<2048>(v1, r1, s1 + (i * 32 + 16) * 3072 - 2048); glds<3072>(v1, r1, s1 + (i * 32 + 24) * 3072 - 3072);
    }
#pragma unroll
    for (int i = 0; i < 2; ++i) {
      setm0((u32)(size_t)lds + C_W2 + i * 4096);
      glds<0>(v2, rw, s2 + i * 16 * 1280); glds<1024>(v2, rw, s2 + (i * 16 + 4) * 1280 - 1024);
      glds<2048>(v2, rw, s2 + (i * 16 + 8) * 1280 - 2048); glds<3072>(v2, rw, s2 + (i * 16 + 12) * 1280 - 3072);
    }
  }
  const int dd = chan ? d : 0;
  const u32 ldt2 = *(const __attribute__((address_space(4))) u32*)(p.logdt + (dd & ~1)), dsk2 = *(const __attribute__((address_space(4))) u32*)(p.dskip + (dd & ~1));
  const rsrc_t rs = mkr(p.ws, WS_END);
  KPrep kp;
  kprep(kp, lane);
  asm volatile("s_waitcnt vmcnt(22)\n\ts_cmp_eq_u32 %5, %6\n\ts_cbranch_scc1 1f\n\ts_waitcnt vmcnt(6)\n1:"
               : "+v"(xa[0]), "+v"(xa[1]), "+v"(xa[2]), "+v"(xa[3]), "+v"(xa[4]) : "s"(w), "i"(3) : "memory", "scc");
  const u32 st = ln_part(xa, lds + C_LN + (tid >> 5) * 1344 + (tid & 31) * 8);
  *(LAS u32*)(lds + C_TAG + 16 + (tid >> 5) * 4) = st;
  __builtin_amdgcn_fence(__ATOMIC_RELEASE, "workgroup", "local");
  __hip_atomic_fetch_add((LAS u32*)(lds + C_TAG + 48), 1u, __ATOMIC_RELAXED, __HIP_MEMORY_SCOPE_WORKGROUP);
  asm volatile("s_waitcnt vmcnt(16)\n\ts_cmp_eq_u32 %6, 3\n\ts_cbranch_scc1 1f\n\ts_waitcnt vmcnt(0)\n1:"
               : "+v"(pq[0]), "+v"(pq[1]), "+v"(pq[2]), "+v"(pq[3]), "+v"(pq[4]), "+v"(pq[5]) : "s"(w) : "memory", "scc");
  if (w == 3) {
    const v2u tag = make_tag(pq[0]);
    *(LAS v2u*)(lds + C_TAG) = tag;
    u32 sw[8];
#pragma unroll
    for (int t = 0; t < 8; ++t) {
      const int T = 8 * cg + t, T5 = (T * 52) >> 8;
      sw[t] = r < 30 ? WS_W2T + (T5 * 235 + ks * 5 + T) * 1024 : WS_END;
    }
#pragma unroll 1
    for (u32 spin = 0; spin < (SPIN_CAP << 4) && *(volatile LAS u32*)(lds + C_TAG + 48) != 256; ++spin) __builtin_amdgcn_s_sleep(1);
    __builtin_amdgcn_fence(__ATOMIC_ACQUIRE, "workgroup", "local");
    publish(rs, lds, c, tb, sw, r < 30 ? WS_CSP + kc * 6144 + strip * 256 : WS_END, tag, lane);
    if (r < 30) {
      if (lane < 30) {
#pragma unroll
        for (int ch = 0; ch < 3; ++ch) *(LAS v4u*)(lds + C_U + ch * C_USZ + lane * 16) = v4u{0, 0, 0, 0};
      }
      waitv<0>();
      __builtin_amdgcn_s_sleep(52);
      wait_flags<2, 24>(rs, lane * 16, WS_FA + xc * 2048, tag);
      gather_u(rs, lds, d0, r - 3 * r3 == 2, lane);
      syncl();
    }
  } else if (chan) {
    LAS unsigned char* T = lds + C_TAB + w * C_TABSZ;
    u16 pr[3][6];
#pragma unroll
    for (int i = 0; i < 6; ++i) {
      const u32 lo = (lane & 1) ? __builtin_amdgcn_alignbit(pq[i].y, pq[i].x, 16) : pq[i].x, hi = (lane & 1) ? pq[i].y >> 16 : pq[i].y;
      pr[0][i] = (u16)lo; pr[1][i] = (u16)(lo >> 16); pr[2][i] = (u16)hi;
    }
    const float ldt = (d & 1) ? bfhi(ldt2) : bflo(ldt2), th = ldt * 1.44269504f;
    const float tl = __builtin_fmaf(ldt, 1.92596299e-8f, __builtin_fmaf(ldt, 1.44269504f, -th));
    const float e0 = __builtin_amdgcn_exp2f(th), dt = rbf(__builtin_fmaf(e0 * tl, 0.69314718f, e0));
    const float dsk = ((d & 1) ? bfhi(dsk2) : bflo(dsk2)) * 0.69314718f;
    kgen(kp, pr, dt, dsk, T, lane);
    syncl();
    __builtin_amdgcn_s_setprio(1);
    const v2u tag = *(LAS v2u*)(lds + C_TAG);
    conv(rs, T, lds + C_U + w * C_USZ, d, lane);
    waitv<0>();
    put_flag2(rs, lane ? WS_END : 0, WS_FS + d * 128, tag);
  }
}

NOPK __global__ void __launch_bounds__(256, 2) k_main(Params p) {
  extern __shared__ __attribute__((aligned(16))) unsigned char smem[];
  LAS unsigned char* lds = (LAS unsigned char*)smem;
  const int tid = __builtin_amdgcn_workitem_id_x(), b = __builtin_amdgcn_workgroup_id_x();
  if (b >= 256) g_role(p, lds, b - 256, tid);
  else c_role(p, lds, b, tid);
}

extern "C" void kernel_launch(void* const* d_in, const int* in_sizes, int n_in, void* d_out, int out_size, void* d_ws, size_t ws_size,
                              hipStream_t stream) {
  Params p;
  p.x = (const u16*)d_in[0]; p.Are = (const u16*)d_in[1]; p.Aim = (const u16*)d_in[2]; p.Bre = (const u16*)d_in[3];
  p.Bim = (const u16*)d_in[4]; p.Cre = (const u16*)d_in[5]; p.Cim = (const u16*)d_in[6]; p.logdt = (const u16*)d_in[7];
  p.dskip = (const u16*)d_in[8]; p.w1 = (const u16*)d_in[10]; p.b1 = (const u16*)d_in[11]; p.w2 = (const u16*)d_in[12];
  p.out = (u16*)d_out; p.ws = (unsigned char*)d_ws;
  (void)hipFuncSetAttribute((const void*)k_main, hipFuncAttributeMaxDynamicSharedMemorySize, LDS_BYTES);
  k_main<<<512, 256, LDS_BYTES, stream>>>(p);
}
```
